```python
import torch

def _softmax(x, axis=-1):
    return torch.softmax(x, dim=axis)
def _gelu(x):
    return torch.nn.functional.gelu(x, approximate="tanh")
def _silu(x):
    return torch.nn.functional.silu(x)
def _sigmoid(x):
    return torch.sigmoid(x)
def _layernorm(x, axis=-1, eps=1e-5):
    m = x.mean(dim=axis, keepdim=True)
    v = x.var(dim=axis, keepdim=True, unbiased=False)
    return (x - m) / torch.sqrt(v + eps)
def _scatter_add(tgt, idx, src, axis=0):
    return tgt.clone().index_add_(axis, idx, src)
def _segment_sum(src, seg, n):
    out = torch.zeros((n,) + src.shape[1:], dtype=src.dtype)
    return out.index_add_(0, seg, src)
def _pad(x, pw):
    flat = tuple(v for pair in reversed(pw) for v in pair)
    return torch.nn.functional.pad(x, flat)

kernel_name = 'concat_heads__gqa_block_cached__gqa_block_cached__near_ridge'

def setup_inputs():

    _w = (lambda a: (lambda t: t.to(torch.bfloat16) if t.is_floating_point() else t)(torch.as_tensor(a)))
    def _A_setup():
        B, Sq, D = 16, 48, 448
        Hq, Hkv, Sk, Dh = 16, 8, 48, 256
        assert Hq % Hkv == 0, (Hq, Hkv)
        return {
            "x": _w((lambda *s: torch.randn(*s, dtype=torch.bfloat16) * 0.8)(B, Sq, D)),
            "k_cache": _w((lambda *s: torch.randn(*s, dtype=torch.bfloat16) * 0.8)(B, Hkv, Sk, Dh)),
            "v_cache": _w((lambda *s: torch.randn(*s, dtype=torch.bfloat16) * 0.05)(B, Hkv, Sk, Dh)),
            "wq": _w((lambda *s: torch.randn(*s, dtype=torch.bfloat16) * 0.8)(D, Hq * Dh) * (D ** -0.5)),
            "wo": _w((lambda *s: torch.randn(*s, dtype=torch.bfloat16) * 0.05)(Hq * Dh, D) * ((Hq * Dh) ** -0.5)),
        }
    _A_in = _A_setup()

    def _B_setup():
        B, Sq, D = 16, 48, 448
        Hq, Hkv, Sk, Dh = 16, 8, 48, 256
        assert Hq % Hkv == 0, (Hq, Hkv)
        return {
            "x": _w((lambda *s: torch.randn(*s, dtype=torch.bfloat16) * 0.8)(B, Sq, D)),
            "k_cache": _w((lambda *s: torch.randn(*s, dtype=torch.bfloat16) * 0.8)(B, Hkv, Sk, Dh)),
            "v_cache": _w((lambda *s: torch.randn(*s, dtype=torch.bfloat16) * 0.05)(B, Hkv, Sk, Dh)),
            "wq": _w((lambda *s: torch.randn(*s, dtype=torch.bfloat16) * 0.8)(D, Hq * Dh) * (D ** -0.5)),
            "wo": _w((lambda *s: torch.randn(*s, dtype=torch.bfloat16) * 0.05)(Hq * Dh, D) * ((Hq * Dh) ** -0.5)),
        }
    _B_in = _B_setup()

    return {
        "A_x": _A_in["x"],
        "A_k_cache": _A_in["k_cache"],
        "A_v_cache": _A_in["v_cache"],
        "A_wq": _A_in["wq"],
        "A_wo": _A_in["wo"],
        "B_x": _B_in["x"],
        "B_k_cache": _B_in["k_cache"],
        "B_v_cache": _B_in["v_cache"],
        "B_wq": _B_in["wq"],
        "B_wo": _B_in["wo"],
    }

def reference(A_x, A_k_cache, A_v_cache, A_wq, A_wo, B_x, B_k_cache, B_v_cache, B_wq, B_wo):
    def _A_ref(x, k_cache, v_cache, wq, wo):
        B, Sq, D = x.shape
        Hq, Dh = 16, 256
        Hkv = k_cache.shape[1]
        rep = Hq // Hkv
        q = (lambda x, *d: x.permute(*d))((x @ wq).reshape(B, Sq, Hq, Dh), 0, 2, 1, 3)
        k = (lambda x, n, axis: torch.repeat_interleave(x, n, dim=axis))(k_cache, rep, axis=1)
        v = (lambda x, n, axis: torch.repeat_interleave(x, n, dim=axis))(v_cache, rep, axis=1)
        scores = torch.einsum("bhqd,bhkd->bhqk", q, k) * (Dh ** -0.5)
        p = _softmax(scores, axis=-1)
        o = torch.einsum("bhqk,bhkd->bhqd", p, v)
        return (lambda x, *d: x.permute(*d))(o, 0, 2, 1, 3).reshape(B, Sq, Hq * Dh) @ wo

    def _B_ref(x, k_cache, v_cache, wq, wo):
        B, Sq, D = x.shape
        Hq, Dh = 16, 256
        Hkv = k_cache.shape[1]
        rep = Hq // Hkv
        q = (lambda x, *d: x.permute(*d))((x @ wq).reshape(B, Sq, Hq, Dh), 0, 2, 1, 3)
        k = (lambda x, n, axis: torch.repeat_interleave(x, n, dim=axis))(k_cache, rep, axis=1)
        v = (lambda x, n, axis: torch.repeat_interleave(x, n, dim=axis))(v_cache, rep, axis=1)
        scores = torch.einsum("bhqd,bhkd->bhqk", q, k) * (Dh ** -0.5)
        p = _softmax(scores, axis=-1)
        o = torch.einsum("bhqk,bhkd->bhqd", p, v)
        return (lambda x, *d: x.permute(*d))(o, 0, 2, 1, 3).reshape(B, Sq, Hq * Dh) @ wo

    a = _A_ref(A_x, A_k_cache, A_v_cache, A_wq, A_wo)
    b = _B_ref(B_x, B_k_cache, B_v_cache, B_wq, B_wo)
    return torch.concatenate([a, b], axis=-1)
```

```cpp
#include <hip/hip_runtime.h>
#include <hip/hip_bf16.h>
#include <stdint.h>


typedef __attribute__((ext_vector_type(8))) short bf16x8;
typedef __attribute__((ext_vector_type(4))) short bf16x4;
typedef __attribute__((ext_vector_type(4))) float f32x4;
typedef __attribute__((ext_vector_type(4))) unsigned u32x4;
typedef __attribute__((ext_vector_type(2))) unsigned u32x2;

__device__ __forceinline__ void glds16(const void* gsrc, const void* ldsptr) {
  unsigned keep;
  unsigned dst = __builtin_amdgcn_readfirstlane((unsigned)(size_t)(__attribute__((address_space(3))) const char*)ldsptr);
  asm volatile("s_mov_b32 %0, m0\n\ts_mov_b32 m0, %2\n\ts_nop 0\n\tglobal_load_lds_dwordx4 %1, off\n\ts_mov_b32 m0, %0"
               : "=&s"(keep) : "v"(gsrc), "s"(dst) : "memory");
}
#define GLDS(gptr, ldsptr) glds16((const void*)(gptr), (const void*)(ldsptr))
#define TRREAD(ldsptr) \
  __builtin_amdgcn_ds_read_tr16_b64_v4i16((__attribute__((address_space(3))) bf16x4*)(ldsptr))
#define MFMA32(a, b, c) __builtin_amdgcn_mfma_f32_16x16x32_bf16((a), (b), (c), 0, 0, 0)
#define MFMA16(a, b, c) __builtin_amdgcn_mfma_f32_16x16x16bf16_1k((a), (b), (c), 0, 0, 0)
#define WAITV(n) asm volatile("s_waitcnt vmcnt(" #n ")" ::: "memory")
#define BARRIER()                     \
  do {                                \
    asm volatile("" ::: "memory");    \
    __builtin_amdgcn_s_barrier();     \
    asm volatile("" ::: "memory");    \
  } while (0)

__device__ __forceinline__ unsigned short f2bf(float f) {
  __hip_bfloat16 h = __float2bfloat16(f);
  return *reinterpret_cast<unsigned short*>(&h);
}
__device__ __forceinline__ float bfround(float f) {
  return __bfloat162float(__float2bfloat16(f));
}
__device__ __forceinline__ unsigned pack2(float a, float b) {
  return (unsigned)f2bf(a) | ((unsigned)f2bf(b) << 16);
}

constexpr int K1_X_OFF = 0;
constexpr int K1_W_OFF = 86016;
constexpr int K1_Q_OFF = 0;
constexpr int K1_K_OFF = 49152;
constexpr int K1_V_OFF = 98304;
constexpr int K1_LDS = 151552;

template <int WAITN>
__device__ __forceinline__ void waitv() {
  if constexpr (WAITN == 16) WAITV(16);
  else if constexpr (WAITN == 12) WAITV(12);
  else if constexpr (WAITN == 8) WAITV(8);
  else if constexpr (WAITN == 4) WAITV(4);
  else if constexpr (WAITN == 2) WAITV(2);
  else WAITV(0);
}

__global__ __launch_bounds__(512, 1) void k1_qproj_attn(
    const unsigned short* __restrict__ xA, const unsigned short* __restrict__ kA,
    const unsigned short* __restrict__ vA, const unsigned short* __restrict__ wqA,
    const unsigned short* __restrict__ xB, const unsigned short* __restrict__ kB,
    const unsigned short* __restrict__ vB, const unsigned short* __restrict__ wqB,
    unsigned short* __restrict__ ows) {
  __shared__ __attribute__((aligned(16))) char lds[K1_LDS];
  const int tid = threadIdx.x;
  const int lane = tid & 63;
  const int w = __builtin_amdgcn_readfirstlane(tid >> 6);
  const int g = lane >> 4, l15 = lane & 15, tq = (lane & 15) >> 2, tp = lane & 3;
  const int wg = blockIdx.x;
  const int xcd = wg & 7, idx = wg >> 3;
  const int blk = xcd >> 2;
  const int h = 4 * (xcd & 3) + (idx & 3);
  const int bp = idx >> 2;
  const unsigned short* x = blk ? xB : xA;
  const unsigned short* kc = blk ? kB : kA;
  const unsigned short* vc = blk ? vB : vA;
  const unsigned short* wq = blk ? wqB : wqA;
  const int kvh = h >> 1;

  {
    const unsigned short* xb = x + (size_t)bp * 96 * 448;
    for (int j = w; j < 84; j += 8) {
      int p = 1024 * j + 16 * lane;
      int row = p / 896;
      int chs = (p - row * 896) >> 4;
      int ch = (chs & ~7) | ((chs ^ (row >> 1)) & 7);
      GLDS(xb + row * 448 + ch * 8, lds + K1_X_OFF + 1024 * j);
    }
  }
  const unsigned short* wbase = wq + h * 256;
  auto load_w = [&](int c, int slot) {
#pragma unroll
    for (int i = 0; i < 2; ++i) {
      int j = 2 * w + i;
      int row = 2 * j + (lane >> 5);
      int f = (row & 3) | (((row >> 3) & 1) << 2);
      int ch = (lane & 31) ^ (f << 1);
      GLDS(wbase + (size_t)(32 * c + row) * 4096 + ch * 8, lds + K1_W_OFF + slot * 16384 + 1024 * j);
    }
  };
  load_w(0, 0);
  load_w(1, 1);
  load_w(2, 2);
  u32x4 kreg[6], vreg[6];
  {
    const unsigned short* kb = kc + ((size_t)(bp * 2) * 8 + kvh) * 48 * 256;
    const unsigned short* vb = vc + ((size_t)(bp * 2) * 8 + kvh) * 48 * 256;
#pragma unroll
    for (int i = 0; i < 6; ++i) {
      int c = tid + 512 * i;
      int bb = c >= 1536 ? 1 : 0;
      int r = c - bb * 1536;
      size_t off = (size_t)bb * 8 * 48 * 256 + (size_t)r * 8;
      kreg[i] = *reinterpret_cast<const u32x4*>(kb + off);
      vreg[i] = *reinterpret_cast<const u32x4*>(vb + off);
    }
  }

  f32x4 acc[2][6];
#pragma unroll
  for (int a = 0; a < 2; ++a)
#pragma unroll
    for (int b = 0; b < 6; ++b) acc[a][b] = f32x4{0.f, 0.f, 0.f, 0.f};

  int a_off[2][2];
#pragma unroll
  for (int dt = 0; dt < 2; ++dt)
#pragma unroll
    for (int t = 0; t < 2; ++t) {
      int row = 8 * g + 4 * t + tq;
      int f = (row & 3) | (((row >> 3) & 1) << 2);
      int c2 = 4 * w + 2 * dt + (tp >> 1);
      a_off[dt][t] = row * 512 + ((c2 ^ (f << 1)) << 4) + 8 * (tp & 1);
    }

  auto compute = [&](int c, int slot) {
    const char* wb = lds + K1_W_OFF + slot * 16384;
    bf16x8 af[2];
#pragma unroll
    for (int dt = 0; dt < 2; ++dt) {
      bf16x4 lo = TRREAD(wb + a_off[dt][0]);
      bf16x4 hi = TRREAD(wb + a_off[dt][1]);
      af[dt] = __builtin_shufflevector(lo, hi, 0, 1, 2, 3, 4, 5, 6, 7);
    }
#pragma unroll
    for (int rt = 0; rt < 6; ++rt) {
      int row = 16 * rt + l15;
      int ch = 4 * c + g;
      int chs = (ch & ~7) | ((ch ^ (row >> 1)) & 7);
      bf16x8 bfr = *reinterpret_cast<const bf16x8*>(lds + K1_X_OFF + row * 896 + chs * 16);
      acc[0][rt] = MFMA32(af[0], bfr, acc[0][rt]);
      acc[1][rt] = MFMA32(af[1], bfr, acc[1][rt]);
    }
  };

#define K1_ITER(i, N)                                   \
  {                                                     \
    waitv<N>();                                         \
    BARRIER();                                          \
    if (i + 3 <= 13) load_w(i + 3, (i + 3) & 3);        \
    compute(i, i & 3);                                  \
  }
  K1_ITER(0, 16) K1_ITER(1, 16) K1_ITER(2, 16) K1_ITER(3, 4) K1_ITER(4, 4) K1_ITER(5, 4)
  K1_ITER(6, 4) K1_ITER(7, 4) K1_ITER(8, 4) K1_ITER(9, 4) K1_ITER(10, 4) K1_ITER(11, 4)
  K1_ITER(12, 2) K1_ITER(13, 0)
#undef K1_ITER

  BARRIER();
#pragma unroll
  for (int dt = 0; dt < 2; ++dt)
#pragma unroll
    for (int rt = 0; rt < 6; ++rt) {
      int row = 16 * rt + l15;
      int chunk = 4 * w + 2 * dt + (g >> 1);
      u32x2 v;
      v[0] = pack2(acc[dt][rt][0], acc[dt][rt][1]);
      v[1] = pack2(acc[dt][rt][2], acc[dt][rt][3]);
      *reinterpret_cast<u32x2*>(lds + K1_Q_OFF + row * 512 + ((chunk ^ (row & 15)) << 4) + 8 * (g & 1)) = v;
    }
#pragma unroll
  for (int i = 0; i < 6; ++i) {
    int c = tid + 512 * i;
    int row = c >> 5;
    int ch = c & 31;
    *reinterpret_cast<u32x4*>(lds + K1_K_OFF + row * 512 + ((ch ^ (row & 15)) << 4)) = kreg[i];
    *reinterpret_cast<u32x4*>(lds + K1_V_OFF + row * 512 + ((ch ^ ((row & 7) << 1)) << 4)) = vreg[i];
  }
  __syncthreads();

  if (w < 6) {
    const int bb = w / 3, qt = w - 3 * bb;
    bf16x8 bq[8];
    {
      int row = 48 * bb + 16 * qt + l15;
#pragma unroll
      for (int s = 0; s < 8; ++s)
        bq[s] = *reinterpret_cast<const bf16x8*>(lds + K1_Q_OFF + row * 512 + (((4 * s + g) ^ (row & 15)) << 4));
    }
    f32x4 sacc[3];
#pragma unroll
    for (int kt = 0; kt < 3; ++kt) {
      sacc[kt] = f32x4{0.f, 0.f, 0.f, 0.f};
      int row = 48 * bb + 16 * kt + l15;
#pragma unroll
      for (int s = 0; s < 8; ++s) {
        bf16x8 ak = *reinterpret_cast<const bf16x8*>(lds + K1_K_OFF + row * 512 + (((4 * s + g) ^ (row & 15)) << 4));
        sacc[kt] = MFMA32(ak, bq[s], sacc[kt]);
      }
    }
    float sv[12];
    float m = -INFINITY;
#pragma unroll
    for (int kt = 0; kt < 3; ++kt)
#pragma unroll
      for (int r = 0; r < 4; ++r) {
        float v = bfround(sacc[kt][r]) * 0.0625f;
        sv[kt * 4 + r] = v;
        m = fmaxf(m, v);
      }
    m = fmaxf(m, __shfl_xor(m, 16));
    m = fmaxf(m, __shfl_xor(m, 32));
    float sum = 0.f;
#pragma unroll
    for (int i = 0; i < 12; ++i) {
      sv[i] = __expf(sv[i] - m);
      sum += sv[i];
    }
    sum += __shfl_xor(sum, 16);
    sum += __shfl_xor(sum, 32);
    float inv = 1.0f / sum;
    u32x4 p0u;
    p0u[0] = pack2(sv[0] * inv, sv[1] * inv);
    p0u[1] = pack2(sv[2] * inv, sv[3] * inv);
    p0u[2] = pack2(sv[4] * inv, sv[5] * inv);
    p0u[3] = pack2(sv[6] * inv, sv[7] * inv);
    u32x2 p1u;
    p1u[0] = pack2(sv[8] * inv, sv[9] * inv);
    p1u[1] = pack2(sv[10] * inv, sv[11] * inv);
    bf16x8 p0 = __builtin_bit_cast(bf16x8, p0u);
    bf16x4 p1 = __builtin_bit_cast(bf16x4, p1u);

    const char* vbase = lds + K1_V_OFF + (48 * bb) * 512;
    int key0 = 4 * g + tq;
    int vsw = (key0 & 7) << 1;
    unsigned short* optr = ows + ((size_t)(blk * 768 + bp * 96 + 48 * bb + 16 * qt + l15)) * 4096 + h * 256 + 4 * g;
#pragma unroll 4
    for (int dt = 0; dt < 16; ++dt) {
      int coff = (((2 * dt + (tp >> 1)) ^ vsw) << 4) + 8 * (tp & 1);
      bf16x4 a0 = TRREAD(vbase + key0 * 512 + coff);
      bf16x4 a1 = TRREAD(vbase + (key0 + 16) * 512 + coff);
      bf16x4 a2 = TRREAD(vbase + (key0 + 32) * 512 + coff);
      bf16x8 a01 = __builtin_shufflevector(a0, a1, 0, 1, 2, 3, 4, 5, 6, 7);
      f32x4 o = f32x4{0.f, 0.f, 0.f, 0.f};
      o = MFMA32(a01, p0, o);
      o = MFMA16(a2, p1, o);
      u32x2 v;
      v[0] = pack2(o[0], o[1]);
      v[1] = pack2(o[2], o[3]);
      *reinterpret_cast<u32x2*>(optr + 16 * dt) = v;
    }
  }
}

constexpr int K2_SLOT = 26624;
constexpr int K2_NSLOT = 5;
constexpr int K2_LDS = K2_SLOT * K2_NSLOT + 2048;

__global__ __launch_bounds__(448, 1) void k2_oproj(
    const unsigned short* __restrict__ ows, const unsigned short* __restrict__ woA,
    const unsigned short* __restrict__ woB, float* __restrict__ part) {
  __shared__ __attribute__((aligned(16))) char lds[K2_LDS];
  const int tid = threadIdx.x;
  const int lane = tid & 63;
  const int w = __builtin_amdgcn_readfirstlane(tid >> 6);
  const int g = lane >> 4, l15 = lane & 15, tq = (lane & 15) >> 2, tp = lane & 3;
  const int wg = blockIdx.x;
  const int xcd = wg & 7, idx = wg >> 3;
  const int blk = xcd >> 2, ks = xcd & 3;
  const int mt = idx & 7, nt = idx >> 3;
  const unsigned short* wo = blk ? woB : woA;
  const unsigned short* ob = ows + (size_t)(blk * 768 + mt * 96) * 4096 + ks * 1024;
  const unsigned short* wb = wo + (size_t)(ks * 1024) * 448 + nt * 112;

  const unsigned short* src[4];
  int dsto[4];
  size_t cstep[4];
#pragma unroll
  for (int i = 0; i < 4; ++i) {
    int j = w + 7 * i;
    int p = 1024 * j + 16 * lane;
    int rowo = p >> 7;
    int cho = ((p >> 4) & 7) ^ (rowo & 7);
    int pw = 1024 * (j - 12) + 16 * lane;
    int roww = pw / 224;
    int chw = (pw - roww * 224) >> 4;
    bool iso = j < 12, isw = (j >= 12) && (j < 26);
    const unsigned short* so = ob + (size_t)rowo * 4096 + cho * 8;
    const unsigned short* sw = wb + (size_t)roww * 448 + chw * 8;
    const unsigned short* sd = ob + (size_t)(lane >> 3) * 4096 + (lane & 7) * 8;
    src[i] = iso ? so : (isw ? sw : sd);
    cstep[i] = isw ? (size_t)64 * 448 : (size_t)64;
    dsto[i] = (iso || isw) ? 1024 * j : (K2_NSLOT * K2_SLOT + 1024 * (j - 26));
  }
  auto load_chunk = [&](int c, int slot) {
#pragma unroll
    for (int i = 0; i < 4; ++i) {
      int d = dsto[i] + ((dsto[i] < K2_NSLOT * K2_SLOT) ? slot * K2_SLOT : 0);
      GLDS(src[i] + cstep[i] * c, lds + d);
    }
  };

  f32x4 acc[6];
#pragma unroll
  for (int b = 0; b < 6; ++b) acc[b] = f32x4{0.f, 0.f, 0.f, 0.f};

  auto compute = [&](int slot) {
    const char* ol = lds + slot * K2_SLOT;
    const char* wl = ol + 12288;
#pragma unroll
    for (int s = 0; s < 2; ++s) {
      int r0 = 32 * s + 8 * g + tq;
      bf16x4 lo = TRREAD(wl + r0 * 224 + 32 * w + 8 * tp);
      bf16x4 hi = TRREAD(wl + (r0 + 4) * 224 + 32 * w + 8 * tp);
      bf16x8 af = __builtin_shufflevector(lo, hi, 0, 1, 2, 3, 4, 5, 6, 7);
#pragma unroll
      for (int rt = 0; rt < 6; ++rt) {
        int row = 16 * rt + l15;
        bf16x8 bfr = *reinterpret_cast<const bf16x8*>(ol + row * 128 + (((4 * s + g) ^ (row & 7)) << 4));
        acc[rt] = MFMA32(af, bfr, acc[rt]);
      }
    }
  };

  load_chunk(0, 0);
  load_chunk(1, 1);
  load_chunk(2, 2);
  load_chunk(3, 3);
#define K2_ITER(i, N)                                          \
  {                                                            \
    waitv<N>();                                                \
    BARRIER();                                                 \
    if (i + 4 <= 15) load_chunk(i + 4, (i + 4) % K2_NSLOT);    \
    compute(i % K2_NSLOT);                                     \
  }
  K2_ITER(0, 12) K2_ITER(1, 12) K2_ITER(2, 12) K2_ITER(3, 12) K2_ITER(4, 12) K2_ITER(5, 12)
  K2_ITER(6, 12) K2_ITER(7, 12) K2_ITER(8, 12) K2_ITER(9, 12) K2_ITER(10, 12) K2_ITER(11, 12)
  K2_ITER(12, 12) K2_ITER(13, 8) K2_ITER(14, 4) K2_ITER(15, 0)
#undef K2_ITER

  float* pp = part + ((size_t)((blk * 4 + ks) * 768 + mt * 96 + l15)) * 448 + nt * 112 + 16 * w + 4 * g;
#pragma unroll
  for (int rt = 0; rt < 6; ++rt) *reinterpret_cast<f32x4*>(pp + (size_t)rt * 16 * 448) = acc[rt];
}

__global__ __launch_bounds__(256) void k3_reduce(const float* __restrict__ part, unsigned short* __restrict__ out) {
  int i = blockIdx.x * 256 + threadIdx.x;
  if (i >= 2 * 768 * 112) return;
  int q = i % 112;
  int row = (i / 112) % 768;
  int blk = i / (112 * 768);
  const float* p = part + ((size_t)(blk * 4) * 768 + row) * 448 + q * 4;
  f32x4 s = *reinterpret_cast<const f32x4*>(p);
#pragma unroll
  for (int k = 1; k < 4; ++k) s += *reinterpret_cast<const f32x4*>(p + (size_t)k * 768 * 448);
  u32x2 v;
  v[0] = pack2(s[0], s[1]);
  v[1] = pack2(s[2], s[3]);
  *reinterpret_cast<u32x2*>(out + (size_t)row * 896 + blk * 448 + q * 4) = v;
}

extern "C" void kernel_launch(void* const* d_in, const int* in_sizes, int n_in, void* d_out, int out_size,
                              void* d_ws, size_t ws_size, hipStream_t stream) {
  const unsigned short* xA = (const unsigned short*)d_in[0];
  const unsigned short* kA = (const unsigned short*)d_in[1];
  const unsigned short* vA = (const unsigned short*)d_in[2];
  const unsigned short* wqA = (const unsigned short*)d_in[3];
  const unsigned short* woA = (const unsigned short*)d_in[4];
  const unsigned short* xB = (const unsigned short*)d_in[5];
  const unsigned short* kB = (const unsigned short*)d_in[6];
  const unsigned short* vB = (const unsigned short*)d_in[7];
  const unsigned short* wqB = (const unsigned short*)d_in[8];
  const unsigned short* woB = (const unsigned short*)d_in[9];
  unsigned short* ows = (unsigned short*)d_ws;
  float* part = (float*)((char*)d_ws + (size_t)2 * 768 * 4096 * 2);
  k1_qproj_attn<<<256, 512, 0, stream>>>(xA, kA, vA, wqA, xB, kB, vB, wqB, ows);
  k2_oproj<<<256, 448, 0, stream>>>(ows, woA, woB, part);
  k3_reduce<<<(2 * 768 * 112 + 255) / 256, 256, 0, stream>>>(part, (unsigned short*)d_out);
}
```

```cpp
#include <hip/hip_runtime.h>
#include <hip/hip_bf16.h>
#include <stdint.h>


typedef __attribute__((ext_vector_type(8))) short bf16x8;
typedef __attribute__((ext_vector_type(4))) short bf16x4;
typedef __attribute__((ext_vector_type(4))) float f32x4;
typedef __attribute__((ext_vector_type(4))) unsigned u32x4;
typedef __attribute__((ext_vector_type(2))) unsigned u32x2;

__device__ __forceinline__ void glds16(const void* gsrc, const void* ldsptr) {
  unsigned keep;
  unsigned dst = __builtin_amdgcn_readfirstlane((unsigned)(size_t)(__attribute__((address_space(3))) const char*)ldsptr);
  asm volatile("s_mov_b32 %0, m0\n\ts_mov_b32 m0, %2\n\ts_nop 0\n\tglobal_load_lds_dwordx4 %1, off\n\ts_mov_b32 m0, %0"
               : "=&s"(keep) : "v"(gsrc), "s"(dst) : "memory");
}
#define GLDS(gptr, ldsptr) glds16((const void*)(gptr), (const void*)(ldsptr))
#define TRREAD(ldsptr) \
  __builtin_amdgcn_ds_read_tr16_b64_v4i16((__attribute__((address_space(3))) bf16x4*)(ldsptr))
#define MFMA32(a, b, c) __builtin_amdgcn_mfma_f32_16x16x32_bf16((a), (b), (c), 0, 0, 0)
#define MFMA16(a, b, c) __builtin_amdgcn_mfma_f32_16x16x16bf16_1k((a), (b), (c), 0, 0, 0)
#define WAITV(n) asm volatile("s_waitcnt vmcnt(" #n ")" ::: "memory")
#define BARRIER()                     \
  do {                                \
    asm volatile("" ::: "memory");    \
    __builtin_amdgcn_s_barrier();     \
    asm volatile("" ::: "memory");    \
  } while (0)

__device__ __forceinline__ unsigned short f2bf(float f) {
  __hip_bfloat16 h = __float2bfloat16(f);
  return *reinterpret_cast<unsigned short*>(&h);
}
__device__ __forceinline__ float bfround(float f) {
  return __bfloat162float(__float2bfloat16(f));
}
__device__ __forceinline__ unsigned pack2(float a, float b) {
  return (unsigned)f2bf(a) | ((unsigned)f2bf(b) << 16);
}

constexpr int K1_REG = 49152;
constexpr int K1_XO = 32768;
constexpr int K1_Q_OFF = 0;
constexpr int K1_K_OFF = K1_REG;
constexpr int K1_V_OFF = 2 * K1_REG;
constexpr int K1_LDS = 3 * K1_REG;

template <int WAITN>
__device__ __forceinline__ void waitv() {
  if constexpr (WAITN == 12) WAITV(12);
  else if constexpr (WAITN == 8) WAITV(8);
  else if constexpr (WAITN == 7) WAITV(7);
  else if constexpr (WAITN == 6) WAITV(6);
  else if constexpr (WAITN == 5) WAITV(5);
  else if constexpr (WAITN == 4) WAITV(4);
  else WAITV(0);
}

__global__ __launch_bounds__(512, 1) void k1_qproj_attn(
    const unsigned short* __restrict__ xA, const unsigned short* __restrict__ kA,
    const unsigned short* __restrict__ vA, const unsigned short* __restrict__ wqA,
    const unsigned short* __restrict__ xB, const unsigned short* __restrict__ kB,
    const unsigned short* __restrict__ vB, const unsigned short* __restrict__ wqB,
    unsigned short* __restrict__ ows, unsigned* __restrict__ cnt) {
  __shared__ __attribute__((aligned(16))) char lds[K1_LDS];
  const int tid = threadIdx.x;
  if (blockIdx.x < 64 && tid == 0) cnt[blockIdx.x * 32] = 0u;
  const int lane = tid & 63;
  const int w = __builtin_amdgcn_readfirstlane(tid >> 6);
  const int g = lane >> 4, l15 = lane & 15, tq = (lane & 15) >> 2, tp = lane & 3;
  const int wg = blockIdx.x;
  const int xcd = wg & 7, idx = wg >> 3;
  const int blk = xcd >> 2;
  const int h = 4 * (xcd & 3) + (idx & 3);
  const int bp = idx >> 2;
  const unsigned short* x = blk ? xB : xA;
  const unsigned short* kc = blk ? kB : kA;
  const unsigned short* vc = blk ? vB : vA;
  const unsigned short* wq = blk ? wqB : wqA;
  const int kvh = h >> 1;

  const unsigned short* srcp[6];
  {
    const unsigned short* wbase = wq + h * 256;
    const unsigned short* xb = x + (size_t)bp * 96 * 448;
#pragma unroll
    for (int i = 0; i < 4; ++i) {
      int row = 2 * (w + 8 * i) + (lane >> 5);
      int f = (row & 3) | (((row >> 3) & 1) << 2);
      int ch = (lane & 31) ^ (f << 1);
      srcp[i] = wbase + (size_t)row * 4096 + ch * 8;
    }
#pragma unroll
    for (int i = 4; i < 6; ++i) {
      int jj = w + 8 * i - 32;
      jj = jj < 12 ? jj : 11;
      int p = 1024 * jj + 16 * lane;
      int row = p >> 7;
      int ch = ((p >> 4) & 7) ^ (row & 7);
      srcp[i] = xb + row * 448 + ch * 8;
    }
  }
  auto load_chunk = [&](int c, int slot) {
#pragma unroll
    for (int i = 0; i < 4; ++i) GLDS(srcp[i] + (size_t)c * 64 * 4096, lds + slot * K1_REG + 1024 * (w + 8 * i));
    GLDS(srcp[4] + c * 64, lds + slot * K1_REG + 1024 * (w + 32));
    if (w < 4) GLDS(srcp[5] + c * 64, lds + slot * K1_REG + 1024 * (w + 40));
  };
  const unsigned short* kb = kc + ((size_t)(bp * 2) * 8 + kvh) * 48 * 256;
  const unsigned short* vb = vc + ((size_t)(bp * 2) * 8 + kvh) * 48 * 256;
  auto load_kv = [&](const unsigned short* base, int region, bool isv) {
#pragma unroll
    for (int i = 0; i < 6; ++i) {
      int j = w + 8 * i;
      int row = 2 * j + (lane >> 5);
      int bb = row >= 48 ? 1 : 0;
      int ch = (lane & 31) ^ (isv ? ((row & 7) << 1) : (row & 15));
      GLDS(base + (size_t)bb * 8 * 48 * 256 + (row - 48 * bb) * 256 + ch * 8, lds + region * K1_REG + 1024 * j);
    }
  };
  load_chunk(0, 0);
  load_chunk(1, 1);
  unsigned dummy[2];
  {
#pragma unroll
    for (int i = 0; i < 2; ++i) {
      int ln = tid + 512 * i;
      ln = ln < 768 ? ln : 767;
      int isv = ln >= 384 ? 1 : 0;
      int r = ln - 384 * isv;
      int bb = r >= 192 ? 1 : 0;
      r -= 192 * bb;
      const unsigned short* pa = (isv ? vb : kb) + (size_t)bb * 8 * 48 * 256 + r * 64;
      asm volatile("global_load_dword %0, %1, off" : "=v"(dummy[i]) : "v"(pa) : "memory");
    }
  }

  f32x4 acc[2][6];
#pragma unroll
  for (int a = 0; a < 2; ++a)
#pragma unroll
    for (int b = 0; b < 6; ++b) acc[a][b] = f32x4{0.f, 0.f, 0.f, 0.f};

  int a_off[2][2];
#pragma unroll
  for (int dt = 0; dt < 2; ++dt)
#pragma unroll
    for (int t = 0; t < 2; ++t) {
      int row = 8 * g + 4 * t + tq;
      int f = (row & 3) | (((row >> 3) & 1) << 2);
      int c2 = 4 * w + 2 * dt + (tp >> 1);
      a_off[dt][t] = row * 512 + ((c2 ^ (f << 1)) << 4) + 8 * (tp & 1);
    }

  auto compute = [&](int slot) {
    const char* wb = lds + slot * K1_REG;
#pragma unroll
    for (int s = 0; s < 2; ++s) {
      bf16x8 af[2];
#pragma unroll
      for (int dt = 0; dt < 2; ++dt) {
        bf16x4 lo = TRREAD(wb + s * 16384 + a_off[dt][0]);
        bf16x4 hi = TRREAD(wb + s * 16384 + a_off[dt][1]);
        af[dt] = __builtin_shufflevector(lo, hi, 0, 1, 2, 3, 4, 5, 6, 7);
      }
#pragma unroll
      for (int rt = 0; rt < 6; ++rt) {
        int row = 16 * rt + l15;
        bf16x8 bfr = *reinterpret_cast<const bf16x8*>(wb + K1_XO + row * 128 + (((4 * s + g) ^ (row & 7)) << 4));
        acc[0][rt] = MFMA32(af[0], bfr, acc[0][rt]);
        acc[1][rt] = MFMA32(af[1], bfr, acc[1][rt]);
      }
    }
  };

#define K1_ITER(i, NA, NB)                                   \
  {                                                          \
    if (w < 4) waitv<NA>(); else waitv<NB>();                \
    BARRIER();                                               \
    if (i + 2 <= 6) load_chunk(i + 2, (i + 2) % 3);          \
    if (i == 5) load_kv(kb, 1, false);                       \
    if (i == 6) load_kv(vb, 2, true);                        \
    compute(i % 3);                                          \
  }
  K1_ITER(0, 8, 7) K1_ITER(1, 8, 7) K1_ITER(2, 6, 5) K1_ITER(3, 6, 5) K1_ITER(4, 6, 5) K1_ITER(5, 6, 5)
  K1_ITER(6, 6, 6)
#undef K1_ITER

  BARRIER();
#pragma unroll
  for (int dt = 0; dt < 2; ++dt)
#pragma unroll
    for (int rt = 0; rt < 6; ++rt) {
      int row = 16 * rt + l15;
      int chunk = 4 * w + 2 * dt + (g >> 1);
      u32x2 v;
      v[0] = pack2(acc[dt][rt][0], acc[dt][rt][1]);
      v[1] = pack2(acc[dt][rt][2], acc[dt][rt][3]);
      *reinterpret_cast<u32x2*>(lds + K1_Q_OFF + row * 512 + ((chunk ^ (row & 15)) << 4) + 8 * (g & 1)) = v;
    }
  WAITV(0);
  asm volatile("" ::"v"(dummy[0]), "v"(dummy[1]));
  __syncthreads();

  if (w < 6) {
    const int bb = w / 3, qt = w - 3 * bb;
    bf16x8 bq[8];
    {
      int row = 48 * bb + 16 * qt + l15;
#pragma unroll
      for (int s = 0; s < 8; ++s)
        bq[s] = *reinterpret_cast<const bf16x8*>(lds + K1_Q_OFF + row * 512 + (((4 * s + g) ^ (row & 15)) << 4));
    }
    f32x4 sacc[3];
#pragma unroll
    for (int kt = 0; kt < 3; ++kt) {
      sacc[kt] = f32x4{0.f, 0.f, 0.f, 0.f};
      int row = 48 * bb + 16 * kt + l15;
#pragma unroll
      for (int s = 0; s < 8; ++s) {
        bf16x8 ak = *reinterpret_cast<const bf16x8*>(lds + K1_K_OFF + row * 512 + (((4 * s + g) ^ (row & 15)) << 4));
        sacc[kt] = MFMA32(ak, bq[s], sacc[kt]);
      }
    }
    float sv[12];
    float m = -INFINITY;
#pragma unroll
    for (int kt = 0; kt < 3; ++kt)
#pragma unroll
      for (int r = 0; r < 4; ++r) {
        float v = bfround(sacc[kt][r]) * 0.0625f;
        sv[kt * 4 + r] = v;
        m = fmaxf(m, v);
      }
    m = fmaxf(m, __shfl_xor(m, 16));
    m = fmaxf(m, __shfl_xor(m, 32));
    float sum = 0.f;
#pragma unroll
    for (int i = 0; i < 12; ++i) {
      sv[i] = __expf(sv[i] - m);
      sum += sv[i];
    }
    sum += __shfl_xor(sum, 16);
    sum += __shfl_xor(sum, 32);
    float inv = 1.0f / sum;
    u32x4 p0u;
    p0u[0] = pack2(sv[0] * inv, sv[1] * inv);
    p0u[1] = pack2(sv[2] * inv, sv[3] * inv);
    p0u[2] = pack2(sv[4] * inv, sv[5] * inv);
    p0u[3] = pack2(sv[6] * inv, sv[7] * inv);
    u32x2 p1u;
    p1u[0] = pack2(sv[8] * inv, sv[9] * inv);
    p1u[1] = pack2(sv[10] * inv, sv[11] * inv);
    bf16x8 p0 = __builtin_bit_cast(bf16x8, p0u);
    bf16x4 p1 = __builtin_bit_cast(bf16x4, p1u);

    const char* vbase = lds + K1_V_OFF + (48 * bb) * 512;
    int key0 = 4 * g + tq;
    int vsw = (key0 & 7) << 1;
    unsigned short* optr = ows + ((size_t)(blk * 768 + bp * 96 + 48 * bb + 16 * qt + l15)) * 4096 + h * 256 + 4 * g;
#pragma unroll 4
    for (int dt = 0; dt < 16; ++dt) {
      int coff = (((2 * dt + (tp >> 1)) ^ vsw) << 4) + 8 * (tp & 1);
      bf16x4 a0 = TRREAD(vbase + key0 * 512 + coff);
      bf16x4 a1 = TRREAD(vbase + (key0 + 16) * 512 + coff);
      bf16x4 a2 = TRREAD(vbase + (key0 + 32) * 512 + coff);
      bf16x8 a01 = __builtin_shufflevector(a0, a1, 0, 1, 2, 3, 4, 5, 6, 7);
      f32x4 o = f32x4{0.f, 0.f, 0.f, 0.f};
      o = MFMA32(a01, p0, o);
      o = MFMA16(a2, p1, o);
      u32x2 v;
      v[0] = pack2(o[0], o[1]);
      v[1] = pack2(o[2], o[3]);
      *reinterpret_cast<u32x2*>(optr + 16 * dt) = v;
    }
  }
}

constexpr int K2_SLOT = 26624;
constexpr int K2_NSLOT = 5;
constexpr int K2_LDS = K2_SLOT * K2_NSLOT + 2048;

__global__ __launch_bounds__(448, 1) void k2_oproj(
    const unsigned short* __restrict__ ows, const unsigned short* __restrict__ woA,
    const unsigned short* __restrict__ woB, float* __restrict__ part, unsigned* __restrict__ cnt,
    unsigned short* __restrict__ out) {
  __shared__ __attribute__((aligned(16))) char lds[K2_LDS];
  const int tid = threadIdx.x;
  const int lane = tid & 63;
  const int w = __builtin_amdgcn_readfirstlane(tid >> 6);
  const int g = lane >> 4, l15 = lane & 15, tq = (lane & 15) >> 2, tp = lane & 3;
  const int wg = blockIdx.x;
  const int xcd = wg & 7, idx = wg >> 3;
  const int blk = xcd >> 2, ks = xcd & 3;
  const int mt = idx & 7, nt = idx >> 3;
  const unsigned short* wo = blk ? woB : woA;
  const unsigned short* ob = ows + (size_t)(blk * 768 + mt * 96) * 4096 + ks * 1024;
  const unsigned short* wb = wo + (size_t)(ks * 1024) * 448 + nt * 112;

  const unsigned short* src[4];
  int dsto[4];
  size_t cstep[4];
#pragma unroll
  for (int i = 0; i < 4; ++i) {
    int j = w + 7 * i;
    int p = 1024 * j + 16 * lane;
    int rowo = p >> 7;
    int cho = ((p >> 4) & 7) ^ (rowo & 7);
    int pw = 1024 * (j - 12) + 16 * lane;
    int roww = pw / 224;
    int chw = (pw - roww * 224) >> 4;
    bool iso = j < 12, isw = (j >= 12) && (j < 26);
    const unsigned short* so = ob + (size_t)rowo * 4096 + cho * 8;
    const unsigned short* sw = wb + (size_t)roww * 448 + chw * 8;
    const unsigned short* sd = ob + (size_t)(lane >> 3) * 4096 + (lane & 7) * 8;
    src[i] = iso ? so : (isw ? sw : sd);
    cstep[i] = isw ? (size_t)64 * 448 : (size_t)64;
    dsto[i] = (iso || isw) ? 1024 * j : (K2_NSLOT * K2_SLOT + 1024 * (j - 26));
  }
  auto load_chunk = [&](int c, int slot) {
#pragma unroll
    for (int i = 0; i < 4; ++i) {
      int d = dsto[i] + ((dsto[i] < K2_NSLOT * K2_SLOT) ? slot * K2_SLOT : 0);
      GLDS(src[i] + cstep[i] * c, lds + d);
    }
  };

  f32x4 acc[6];
#pragma unroll
  for (int b = 0; b < 6; ++b) acc[b] = f32x4{0.f, 0.f, 0.f, 0.f};

  auto compute = [&](int slot) {
    const char* ol = lds + slot * K2_SLOT;
    const char* wl = ol + 12288;
#pragma unroll
    for (int s = 0; s < 2; ++s) {
      int r0 = 32 * s + 8 * g + tq;
      bf16x4 lo = TRREAD(wl + r0 * 224 + 32 * w + 8 * tp);
      bf16x4 hi = TRREAD(wl + (r0 + 4) * 224 + 32 * w + 8 * tp);
      bf16x8 af = __builtin_shufflevector(lo, hi, 0, 1, 2, 3, 4, 5, 6, 7);
#pragma unroll
      for (int rt = 0; rt < 6; ++rt) {
        int row = 16 * rt + l15;
        bf16x8 bfr = *reinterpret_cast<const bf16x8*>(ol + row * 128 + (((4 * s + g) ^ (row & 7)) << 4));
        acc[rt] = MFMA32(af, bfr, acc[rt]);
      }
    }
  };

  load_chunk(0, 0);
  load_chunk(1, 1);
  load_chunk(2, 2);
  load_chunk(3, 3);
#define K2_ITER(i, N)                                          \
  {                                                            \
    waitv<N>();                                                \
    BARRIER();                                                 \
    if (i + 4 <= 15) load_chunk(i + 4, (i + 4) % K2_NSLOT);    \
    compute(i % K2_NSLOT);                                     \
  }
  K2_ITER(0, 12) K2_ITER(1, 12) K2_ITER(2, 12) K2_ITER(3, 12) K2_ITER(4, 12) K2_ITER(5, 12)
  K2_ITER(6, 12) K2_ITER(7, 12) K2_ITER(8, 12) K2_ITER(9, 12) K2_ITER(10, 12) K2_ITER(11, 12)
  K2_ITER(12, 12) K2_ITER(13, 8) K2_ITER(14, 4) K2_ITER(15, 0)
#undef K2_ITER

  const size_t slab = (size_t)768 * 448;
  float* pp = part + ((size_t)(blk * 4) * 768 + mt * 96 + l15) * 448 + nt * 112 + 16 * w + 4 * g;
#pragma unroll
  for (int rt = 0; rt < 6; ++rt)
    asm volatile("global_store_dwordx4 %0, %1, off sc1\n\ts_nop 1" ::"v"(pp + ks * slab + (size_t)rt * 16 * 448), "v"(acc[rt]) : "memory");
  WAITV(0);
  __syncthreads();
  unsigned* flag = reinterpret_cast<unsigned*>(lds);
  if (tid == 0) {
    unsigned old = __hip_atomic_fetch_add(cnt + ((blk * 8 + mt) * 4 + nt) * 32, 1u, __ATOMIC_RELAXED, __HIP_MEMORY_SCOPE_AGENT);
    if (old == 3u) {
      __builtin_amdgcn_fence(__ATOMIC_ACQUIRE, "agent");
      WAITV(0);
    }
    *flag = old;
  }
  __syncthreads();
  if (*flag == 3u) {
    unsigned short* op = out + (size_t)(mt * 96 + l15) * 896 + blk * 448 + nt * 112 + 16 * w + 4 * g;
#pragma unroll
    for (int rt = 0; rt < 6; ++rt) {
      f32x4 sum = f32x4{0.f, 0.f, 0.f, 0.f};
#pragma unroll
      for (int k = 0; k < 4; ++k) sum += *reinterpret_cast<const f32x4*>(pp + k * slab + (size_t)rt * 16 * 448);
      u32x2 v;
      v[0] = pack2(sum[0], sum[1]);
      v[1] = pack2(sum[2], sum[3]);
      *reinterpret_cast<u32x2*>(op + (size_t)rt * 16 * 896) = v;
    }
  }
}

extern "C" void kernel_launch(void* const* d_in, const int* in_sizes, int n_in, void* d_out, int out_size,
                              void* d_ws, size_t ws_size, hipStream_t stream) {
  const unsigned short* xA = (const unsigned short*)d_in[0];
  const unsigned short* kA = (const unsigned short*)d_in[1];
  const unsigned short* vA = (const unsigned short*)d_in[2];
  const unsigned short* wqA = (const unsigned short*)d_in[3];
  const unsigned short* woA = (const unsigned short*)d_in[4];
  const unsigned short* xB = (const unsigned short*)d_in[5];
  const unsigned short* kB = (const unsigned short*)d_in[6];
  const unsigned short* vB = (const unsigned short*)d_in[7];
  const unsigned short* wqB = (const unsigned short*)d_in[8];
  const unsigned short* woB = (const unsigned short*)d_in[9];
  unsigned short* ows = (unsigned short*)d_ws;
  float* part = (float*)((char*)d_ws + (size_t)2 * 768 * 4096 * 2);
  unsigned* cnt = (unsigned*)((char*)d_ws + (size_t)64 * 1024 * 1024);
  k1_qproj_attn<<<256, 512, 0, stream>>>(xA, kA, vA, wqA, xB, kB, vB, wqB, ows, cnt);
  k2_oproj<<<256, 448, 0, stream>>>(ows, woA, woB, part, cnt, (unsigned short*)d_out);
}
```

```cpp
#include <hip/hip_runtime.h>
#include <hip/hip_bf16.h>
#include <stdint.h>


typedef __attribute__((ext_vector_type(8))) short bf16x8;
typedef __attribute__((ext_vector_type(4))) short bf16x4;
typedef __attribute__((ext_vector_type(4))) float f32x4;
typedef __attribute__((ext_vector_type(4))) unsigned u32x4;
typedef __attribute__((ext_vector_type(2))) unsigned u32x2;

__device__ __forceinline__ void glds16(const void* gsrc, const void* ldsptr) {
  unsigned keep;
  unsigned dst = __builtin_amdgcn_readfirstlane((unsigned)(size_t)(__attribute__((address_space(3))) const char*)ldsptr);
  asm volatile("s_mov_b32 %0, m0\n\ts_mov_b32 m0, %2\n\ts_nop 0\n\tglobal_load_lds_dwordx4 %1, off\n\ts_mov_b32 m0, %0"
               : "=&s"(keep) : "v"(gsrc), "s"(dst) : "memory");
}
#define GLDS(gptr, ldsptr) glds16((const void*)(gptr), (const void*)(ldsptr))
#define TRREAD(ldsptr) \
  __builtin_amdgcn_ds_read_tr16_b64_v4i16((__attribute__((address_space(3))) bf16x4*)(ldsptr))
#define MFMA32(a, b, c) __builtin_amdgcn_mfma_f32_16x16x32_bf16((a), (b), (c), 0, 0, 0)
#define MFMA16(a, b, c) __builtin_amdgcn_mfma_f32_16x16x16bf16_1k((a), (b), (c), 0, 0, 0)
#define WAITV(n) asm volatile("s_waitcnt vmcnt(" #n ")" ::: "memory")
#define BARRIER()                     \
  do {                                \
    asm volatile("" ::: "memory");    \
    __builtin_amdgcn_s_barrier();     \
    asm volatile("" ::: "memory");    \
  } while (0)

__device__ __forceinline__ unsigned short f2bf(float f) {
  __hip_bfloat16 h = __float2bfloat16(f);
  return *reinterpret_cast<unsigned short*>(&h);
}
__device__ __forceinline__ float bfround(float f) {
  return __bfloat162float(__float2bfloat16(f));
}
__device__ __forceinline__ unsigned pack2(float a, float b) {
  return (unsigned)f2bf(a) | ((unsigned)f2bf(b) << 16);
}

constexpr int K1_REG = 49152;
constexpr int K1_XO = 32768;
constexpr int K1_Q_OFF = 0;
constexpr int K1_K_OFF = K1_REG;
constexpr int K1_V_OFF = 2 * K1_REG;
constexpr int K1_LDS = 3 * K1_REG;

template <int WAITN>
__device__ __forceinline__ void waitv() {
  if constexpr (WAITN == 12) WAITV(12);
  else if constexpr (WAITN == 11) WAITV(11);
  else if constexpr (WAITN == 8) WAITV(8);
  else if constexpr (WAITN == 7) WAITV(7);
  else if constexpr (WAITN == 6) WAITV(6);
  else if constexpr (WAITN == 5) WAITV(5);
  else if constexpr (WAITN == 4) WAITV(4);
  else WAITV(0);
}

__global__ __launch_bounds__(512, 1) void k1_qproj_attn(
    const unsigned short* __restrict__ xA, const unsigned short* __restrict__ kA,
    const unsigned short* __restrict__ vA, const unsigned short* __restrict__ wqA,
    const unsigned short* __restrict__ xB, const unsigned short* __restrict__ kB,
    const unsigned short* __restrict__ vB, const unsigned short* __restrict__ wqB,
    unsigned short* __restrict__ ows) {
  __shared__ __attribute__((aligned(16))) char lds[K1_LDS];
  const int tid = threadIdx.x;
  const int lane = tid & 63;
  const int w = __builtin_amdgcn_readfirstlane(tid >> 6);
  const int g = lane >> 4, l15 = lane & 15, tq = (lane & 15) >> 2, tp = lane & 3;
  const int wg = blockIdx.x;
  const int xcd = wg & 7, idx = wg >> 3;
  const int blk = xcd >> 2;
  const int h = 4 * (xcd & 3) + (idx & 3);
  const int bp = idx >> 2;
  const unsigned short* x = blk ? xB : xA;
  const unsigned short* kc = blk ? kB : kA;
  const unsigned short* vc = blk ? vB : vA;
  const unsigned short* wq = blk ? wqB : wqA;
  const int kvh = h >> 1;

  const int lw = w - 4;
  const unsigned short* kb = kc + ((size_t)(bp * 2) * 8 + kvh) * 48 * 256;
  const unsigned short* vb = vc + ((size_t)(bp * 2) * 8 + kvh) * 48 * 256;
  f32x4 acc[4][6];
#pragma unroll
  for (int a = 0; a < 4; ++a)
#pragma unroll
    for (int b = 0; b < 6; ++b) acc[a][b] = f32x4{0.f, 0.f, 0.f, 0.f};

  if (w >= 4) {
    const unsigned short* wsrc;
    const unsigned short* xsrc;
    {
      int row = 2 * lw + (lane >> 5);
      int f = (row & 3);
      wsrc = wq + h * 256 + (size_t)row * 4096;
      (void)f;
      int p = 1024 * lw + 16 * lane;
      int xr = p >> 7;
      xsrc = x + (size_t)bp * 96 * 448 + xr * 448 + ((((p >> 4) & 7) ^ (xr & 7)) << 3);
    }
    auto load_chunk = [&](int c, int slot) {
#pragma unroll
      for (int i = 0; i < 8; ++i) {
        int row = 2 * (lw + 4 * i) + (lane >> 5);
        int f = (row & 3) | (((row >> 3) & 1) << 2);
        int ch = (lane & 31) ^ (f << 1);
        GLDS(wsrc + (size_t)(c * 64 + 8 * i) * 4096 + ch * 8, lds + slot * K1_REG + 1024 * (lw + 4 * i));
      }
#pragma unroll
      for (int i = 0; i < 3; ++i) GLDS(xsrc + (size_t)i * 32 * 448 + c * 64, lds + slot * K1_REG + K1_XO + 1024 * (lw + 4 * i));
    };
    auto load_kv = [&](const unsigned short* base, int region, bool isv) {
#pragma unroll
      for (int i = 0; i < 12; ++i) {
        int j = lw + 4 * i;
        int row = 2 * j + (lane >> 5);
        int bb = row >= 48 ? 1 : 0;
        int ch = (lane & 31) ^ (isv ? ((row & 7) << 1) : (row & 15));
        GLDS(base + (size_t)bb * 8 * 48 * 256 + (row - 48 * bb) * 256 + ch * 8, lds + region * K1_REG + 1024 * j);
      }
    };
    load_chunk(0, 0);
    load_chunk(1, 1);
#define K1_LITER(i, N)                                  \
  {                                                     \
    waitv<N>();                                         \
    BARRIER();                                          \
    if (i + 2 <= 6) load_chunk(i + 2, (i + 2) % 3);     \
    if (i == 5) load_kv(kb, 1, false);                  \
    if (i == 6) load_kv(vb, 2, true);                   \
  }
    K1_LITER(0, 11) K1_LITER(1, 11) K1_LITER(2, 11) K1_LITER(3, 11) K1_LITER(4, 11) K1_LITER(5, 11) K1_LITER(6, 12)
#undef K1_LITER
  } else {
    int a_off[2];
#pragma unroll
    for (int t = 0; t < 2; ++t) {
      int row = 8 * g + 4 * t + tq;
      int f = (row & 3) | (((row >> 3) & 1) << 2);
      int c2 = 8 * w + (tp >> 1);
      a_off[t] = row * 512 + ((c2 ^ (f << 1)) << 4) + 8 * (tp & 1);
    }
    auto compute = [&](int slot) {
      const char* wb = lds + slot * K1_REG;
#pragma unroll
      for (int s = 0; s < 2; ++s) {
        bf16x8 af[4];
#pragma unroll
        for (int dt = 0; dt < 4; ++dt) {
          bf16x4 lo = TRREAD(wb + s * 16384 + (a_off[0] ^ (dt << 5)));
          bf16x4 hi = TRREAD(wb + s * 16384 + (a_off[1] ^ (dt << 5)));
          af[dt] = __builtin_shufflevector(lo, hi, 0, 1, 2, 3, 4, 5, 6, 7);
        }
#pragma unroll
        for (int rt = 0; rt < 6; ++rt) {
          int row = 16 * rt + l15;
          bf16x8 bfr = *reinterpret_cast<const bf16x8*>(wb + K1_XO + row * 128 + (((4 * s + g) ^ (row & 7)) << 4));
#pragma unroll
          for (int dt = 0; dt < 4; ++dt) acc[dt][rt] = MFMA32(af[dt], bfr, acc[dt][rt]);
        }
      }
    };
#pragma unroll
    for (int i = 0; i < 7; ++i) {
      BARRIER();
      compute(i % 3);
    }
  }

  BARRIER();
  if (w < 4) {
#pragma unroll
    for (int dt = 0; dt < 4; ++dt)
#pragma unroll
      for (int rt = 0; rt < 6; ++rt) {
        int row = 16 * rt + l15;
        int chunk = 8 * w + 2 * dt + (g >> 1);
        u32x2 v;
        v[0] = pack2(acc[dt][rt][0], acc[dt][rt][1]);
        v[1] = pack2(acc[dt][rt][2], acc[dt][rt][3]);
        *reinterpret_cast<u32x2*>(lds + K1_Q_OFF + row * 512 + ((chunk ^ (row & 15)) << 4) + 8 * (g & 1)) = v;
      }
  }
  WAITV(0);
  __syncthreads();

  if (w < 6) {
    const int bb = w / 3, qt = w - 3 * bb;
    bf16x8 bq[8];
    {
      int row = 48 * bb + 16 * qt + l15;
#pragma unroll
      for (int s = 0; s < 8; ++s)
        bq[s] = *reinterpret_cast<const bf16x8*>(lds + K1_Q_OFF + row * 512 + (((4 * s + g) ^ (row & 15)) << 4));
    }
    f32x4 sacc[3];
#pragma unroll
    for (int kt = 0; kt < 3; ++kt) {
      sacc[kt] = f32x4{0.f, 0.f, 0.f, 0.f};
      int row = 48 * bb + 16 * kt + l15;
#pragma unroll
      for (int s = 0; s < 8; ++s) {
        bf16x8 ak = *reinterpret_cast<const bf16x8*>(lds + K1_K_OFF + row * 512 + (((4 * s + g) ^ (row & 15)) << 4));
        sacc[kt] = MFMA32(ak, bq[s], sacc[kt]);
      }
    }
    float sv[12];
    float m = -INFINITY;
#pragma unroll
    for (int kt = 0; kt < 3; ++kt)
#pragma unroll
      for (int r = 0; r < 4; ++r) {
        float v = bfround(sacc[kt][r]) * 0.0625f;
        sv[kt * 4 + r] = v;
        m = fmaxf(m, v);
      }
    m = fmaxf(m, __shfl_xor(m, 16));
    m = fmaxf(m, __shfl_xor(m, 32));
    float sum = 0.f;
#pragma unroll
    for (int i = 0; i < 12; ++i) {
      sv[i] = __expf(sv[i] - m);
      sum += sv[i];
    }
    sum += __shfl_xor(sum, 16);
    sum += __shfl_xor(sum, 32);
    float inv = 1.0f / sum;
    u32x4 p0u;
    p0u[0] = pack2(sv[0] * inv, sv[1] * inv);
    p0u[1] = pack2(sv[2] * inv, sv[3] * inv);
    p0u[2] = pack2(sv[4] * inv, sv[5] * inv);
    p0u[3] = pack2(sv[6] * inv, sv[7] * inv);
    u32x2 p1u;
    p1u[0] = pack2(sv[8] * inv, sv[9] * inv);
    p1u[1] = pack2(sv[10] * inv, sv[11] * inv);
    bf16x8 p0 = __builtin_bit_cast(bf16x8, p0u);
    bf16x4 p1 = __builtin_bit_cast(bf16x4, p1u);

    const char* vbase = lds + K1_V_OFF + (48 * bb) * 512;
    int key0 = 4 * g + tq;
    int vsw = (key0 & 7) << 1;
    unsigned short* optr = ows + ((size_t)(blk * 768 + bp * 96 + 48 * bb + 16 * qt + l15)) * 4096 + h * 256 + 4 * g;
#pragma unroll 4
    for (int dt = 0; dt < 16; ++dt) {
      int coff = (((2 * dt + (tp >> 1)) ^ vsw) << 4) + 8 * (tp & 1);
      bf16x4 a0 = TRREAD(vbase + key0 * 512 + coff);
      bf16x4 a1 = TRREAD(vbase + (key0 + 16) * 512 + coff);
      bf16x4 a2 = TRREAD(vbase + (key0 + 32) * 512 + coff);
      bf16x8 a01 = __builtin_shufflevector(a0, a1, 0, 1, 2, 3, 4, 5, 6, 7);
      f32x4 o = f32x4{0.f, 0.f, 0.f, 0.f};
      o = MFMA32(a01, p0, o);
      o = MFMA16(a2, p1, o);
      u32x2 v;
      v[0] = pack2(o[0], o[1]);
      v[1] = pack2(o[2], o[3]);
      *reinterpret_cast<u32x2*>(optr + 16 * dt) = v;
    }
  }
}

constexpr int K2_SLOT = 26624;
constexpr int K2_NSLOT = 5;
constexpr int K2_LDS = K2_SLOT * K2_NSLOT + 2048;

__global__ __launch_bounds__(448, 1) void k2_oproj(
    const unsigned short* __restrict__ ows, const unsigned short* __restrict__ woA,
    const unsigned short* __restrict__ woB, float* __restrict__ part) {
  __shared__ __attribute__((aligned(16))) char lds[K2_LDS];
  const int tid = threadIdx.x;
  const int lane = tid & 63;
  const int w = __builtin_amdgcn_readfirstlane(tid >> 6);
  const int g = lane >> 4, l15 = lane & 15, tq = (lane & 15) >> 2, tp = lane & 3;
  const int wg = blockIdx.x;
  const int xcd = wg & 7, idx = wg >> 3;
  const int blk = xcd >> 2, ks = xcd & 3;
  const int mt = idx & 7, nt = idx >> 3;
  const unsigned short* wo = blk ? woB : woA;
  const unsigned short* ob = ows + (size_t)(blk * 768 + mt * 96) * 4096 + ks * 1024;
  const unsigned short* wb = wo + (size_t)(ks * 1024) * 448 + nt * 112;

  const unsigned short* src[4];
  int dsto[4];
  size_t cstep[4];
#pragma unroll
  for (int i = 0; i < 4; ++i) {
    int j = w + 7 * i;
    int p = 1024 * j + 16 * lane;
    int rowo = p >> 7;
    int cho = ((p >> 4) & 7) ^ (rowo & 7);
    int pw = 1024 * (j - 12) + 16 * lane;
    int roww = pw / 224;
    int chw = (pw - roww * 224) >> 4;
    bool iso = j < 12, isw = (j >= 12) && (j < 26);
    const unsigned short* so = ob + (size_t)rowo * 4096 + cho * 8;
    const unsigned short* sw = wb + (size_t)roww * 448 + chw * 8;
    const unsigned short* sd = ob + (size_t)(lane >> 3) * 4096 + (lane & 7) * 8;
    src[i] = iso ? so : (isw ? sw : sd);
    cstep[i] = isw ? (size_t)64 * 448 : (size_t)64;
    dsto[i] = (iso || isw) ? 1024 * j : (K2_NSLOT * K2_SLOT + 1024 * (j - 26));
  }
  auto load_chunk = [&](int c, int slot) {
#pragma unroll
    for (int i = 0; i < 4; ++i) {
      int d = dsto[i] + ((dsto[i] < K2_NSLOT * K2_SLOT) ? slot * K2_SLOT : 0);
      GLDS(src[i] + cstep[i] * c, lds + d);
    }
  };

  f32x4 acc[6];
#pragma unroll
  for (int b = 0; b < 6; ++b) acc[b] = f32x4{0.f, 0.f, 0.f, 0.f};

  auto compute = [&](int slot) {
    const char* ol = lds + slot * K2_SLOT;
    const char* wl = ol + 12288;
#pragma unroll
    for (int s = 0; s < 2; ++s) {
      int r0 = 32 * s + 8 * g + tq;
      bf16x4 lo = TRREAD(wl + r0 * 224 + 32 * w + 8 * tp);
      bf16x4 hi = TRREAD(wl + (r0 + 4) * 224 + 32 * w + 8 * tp);
      bf16x8 af = __builtin_shufflevector(lo, hi, 0, 1, 2, 3, 4, 5, 6, 7);
#pragma unroll
      for (int rt = 0; rt < 6; ++rt) {
        int row = 16 * rt + l15;
        bf16x8 bfr = *reinterpret_cast<const bf16x8*>(ol + row * 128 + (((4 * s + g) ^ (row & 7)) << 4));
        acc[rt] = MFMA32(af, bfr, acc[rt]);
      }
    }
  };

  load_chunk(0, 0);
  load_chunk(1, 1);
  load_chunk(2, 2);
  load_chunk(3, 3);
#define K2_ITER(i, N)                                          \
  {                                                            \
    waitv<N>();                                                \
    BARRIER();                                                 \
    if (i + 4 <= 15) load_chunk(i + 4, (i + 4) % K2_NSLOT);    \
    compute(i % K2_NSLOT);                                     \
  }
  K2_ITER(0, 12) K2_ITER(1, 12) K2_ITER(2, 12) K2_ITER(3, 12) K2_ITER(4, 12) K2_ITER(5, 12)
  K2_ITER(6, 12) K2_ITER(7, 12) K2_ITER(8, 12) K2_ITER(9, 12) K2_ITER(10, 12) K2_ITER(11, 12)
  K2_ITER(12, 12) K2_ITER(13, 8) K2_ITER(14, 4) K2_ITER(15, 0)
#undef K2_ITER

  float* pp = part + ((size_t)((blk * 4 + ks) * 768 + mt * 96 + l15)) * 448 + nt * 112 + 16 * w + 4 * g;
#pragma unroll
  for (int rt = 0; rt < 6; ++rt) *reinterpret_cast<f32x4*>(pp + (size_t)rt * 16 * 448) = acc[rt];
}

__global__ __launch_bounds__(256) void k3_reduce(const float* __restrict__ part, unsigned short* __restrict__ out) {
  int i = blockIdx.x * 256 + threadIdx.x;
  if (i >= 2 * 768 * 112) return;
  int q = i % 112;
  int row = (i / 112) % 768;
  int blk = i / (112 * 768);
  const float* p = part + ((size_t)(blk * 4) * 768 + row) * 448 + q * 4;
  f32x4 s = *reinterpret_cast<const f32x4*>(p);
#pragma unroll
  for (int k = 1; k < 4; ++k) s += *reinterpret_cast<const f32x4*>(p + (size_t)k * 768 * 448);
  u32x2 v;
  v[0] = pack2(s[0], s[1]);
  v[1] = pack2(s[2], s[3]);
  *reinterpret_cast<u32x2*>(out + (size_t)row * 896 + blk * 448 + q * 4) = v;
}

extern "C" void kernel_launch(void* const* d_in, const int* in_sizes, int n_in, void* d_out, int out_size,
                              void* d_ws, size_t ws_size, hipStream_t stream) {
  const unsigned short* xA = (const unsigned short*)d_in[0];
  const unsigned short* kA = (const unsigned short*)d_in[1];
  const unsigned short* vA = (const unsigned short*)d_in[2];
  const unsigned short* wqA = (const unsigned short*)d_in[3];
  const unsigned short* woA = (const unsigned short*)d_in[4];
  const unsigned short* xB = (const unsigned short*)d_in[5];
  const unsigned short* kB = (const unsigned short*)d_in[6];
  const unsigned short* vB = (const unsigned short*)d_in[7];
  const unsigned short* wqB = (const unsigned short*)d_in[8];
  const unsigned short* woB = (const unsigned short*)d_in[9];
  unsigned short* ows = (unsigned short*)d_ws;
  float* part = (float*)((char*)d_ws + (size_t)2 * 768 * 4096 * 2);
  k1_qproj_attn<<<256, 512, 0, stream>>>(xA, kA, vA, wqA, xB, kB, vB, wqB, ows);
  k2_oproj<<<256, 448, 0, stream>>>(ows, woA, woB, part);
  k3_reduce<<<(2 * 768 * 112 + 255) / 256, 256, 0, stream>>>(part, (unsigned short*)d_out);
}
```

```cpp
#include <hip/hip_runtime.h>
#include <hip/hip_bf16.h>
#include <stdint.h>


typedef __attribute__((ext_vector_type(8))) short bf16x8;
typedef __attribute__((ext_vector_type(4))) short bf16x4;
typedef __attribute__((ext_vector_type(4))) float f32x4;
typedef __attribute__((ext_vector_type(4))) unsigned u32x4;
typedef __attribute__((ext_vector_type(2))) unsigned u32x2;

__device__ __forceinline__ void glds16(const void* gsrc, const void* ldsptr) {
  unsigned keep;
  unsigned dst = __builtin_amdgcn_readfirstlane((unsigned)(size_t)(__attribute__((address_space(3))) const char*)ldsptr);
  asm volatile("s_mov_b32 %0, m0\n\ts_mov_b32 m0, %2\n\ts_nop 0\n\tglobal_load_lds_dwordx4 %1, off\n\ts_mov_b32 m0, %0"
               : "=&s"(keep) : "v"(gsrc), "s"(dst) : "memory");
}
#define GLDS(gptr, ldsptr) glds16((const void*)(gptr), (const void*)(ldsptr))
#define TRREAD(ldsptr) \
  __builtin_amdgcn_ds_read_tr16_b64_v4i16((__attribute__((address_space(3))) bf16x4*)(ldsptr))
#define MFMA32(a, b, c) __builtin_amdgcn_mfma_f32_16x16x32_bf16((a), (b), (c), 0, 0, 0)
#define MFMA16(a, b, c) __builtin_amdgcn_mfma_f32_16x16x16bf16_1k((a), (b), (c), 0, 0, 0)
#define WAITV(n) asm volatile("s_waitcnt vmcnt(" #n ")" ::: "memory")
#define BARRIER()                     \
  do {                                \
    asm volatile("" ::: "memory");    \
    __builtin_amdgcn_s_barrier();     \
    asm volatile("" ::: "memory");    \
  } while (0)

__device__ __forceinline__ unsigned short f2bf(float f) {
  __hip_bfloat16 h = __float2bfloat16(f);
  return *reinterpret_cast<unsigned short*>(&h);
}
__device__ __forceinline__ float bfround(float f) {
  return __bfloat162float(__float2bfloat16(f));
}
__device__ __forceinline__ unsigned pack2(float a, float b) {
  return (unsigned)f2bf(a) | ((unsigned)f2bf(b) << 16);
}

constexpr int K1_REG = 49152;
constexpr int K1_XO = 32768;
constexpr int K1_Q_OFF = 0;
constexpr int K1_K_OFF = K1_REG;
constexpr int K1_V_OFF = 2 * K1_REG;
constexpr int K1_LDS = 3 * K1_REG;

template <int WAITN>
__device__ __forceinline__ void waitv() {
  if constexpr (WAITN == 12) WAITV(12);
  else if constexpr (WAITN == 11) WAITV(11);
  else if constexpr (WAITN == 8) WAITV(8);
  else if constexpr (WAITN == 7) WAITV(7);
  else if constexpr (WAITN == 6) WAITV(6);
  else if constexpr (WAITN == 5) WAITV(5);
  else if constexpr (WAITN == 4) WAITV(4);
  else WAITV(0);
}

__global__ __launch_bounds__(512, 1) void k1_qproj_attn(
    const unsigned short* __restrict__ xA, const unsigned short* __restrict__ kA,
    const unsigned short* __restrict__ vA, const unsigned short* __restrict__ wqA,
    const unsigned short* __restrict__ xB, const unsigned short* __restrict__ kB,
    const unsigned short* __restrict__ vB, const unsigned short* __restrict__ wqB,
    unsigned short* __restrict__ ows) {
  __shared__ __attribute__((aligned(16))) char lds[K1_LDS];
  const int tid = threadIdx.x;
  const int lane = tid & 63;
  const int w = __builtin_amdgcn_readfirstlane(tid >> 6);
  const int g = lane >> 4, l15 = lane & 15, tq = (lane & 15) >> 2, tp = lane & 3;
  const int wg = blockIdx.x;
  const int xcd = wg & 7, idx = wg >> 3;
  const int blk = xcd >> 2;
  const int h = 4 * (xcd & 3) + (idx & 3);
  const int bp = idx >> 2;
  const unsigned short* x = blk ? xB : xA;
  const unsigned short* kc = blk ? kB : kA;
  const unsigned short* vc = blk ? vB : vA;
  const unsigned short* wq = blk ? wqB : wqA;
  const int kvh = h >> 1;

  const int lw = w - 4;
  const unsigned short* kb = kc + ((size_t)(bp * 2) * 8 + kvh) * 48 * 256;
  const unsigned short* vb = vc + ((size_t)(bp * 2) * 8 + kvh) * 48 * 256;
  f32x4 acc[4][6];
#pragma unroll
  for (int a = 0; a < 4; ++a)
#pragma unroll
    for (int b = 0; b < 6; ++b) acc[a][b] = f32x4{0.f, 0.f, 0.f, 0.f};

  if (w >= 4) {
    const unsigned short* wsrc;
    const unsigned short* xsrc;
    {
      int row = 2 * lw + (lane >> 5);
      int f = (row & 3);
      wsrc = wq + h * 256 + (size_t)row * 4096;
      (void)f;
      int p = 1024 * lw + 16 * lane;
      int xr = p >> 7;
      xsrc = x + (size_t)bp * 96 * 448 + xr * 448 + ((((p >> 4) & 7) ^ (xr & 7)) << 3);
    }
    auto load_chunk = [&](int c, int slot) {
#pragma unroll
      for (int i = 0; i < 8; ++i) {
        int row = 2 * (lw + 4 * i) + (lane >> 5);
        int f = (row & 3) | (((row >> 3) & 1) << 2);
        int ch = (lane & 31) ^ (f << 1);
        GLDS(wsrc + (size_t)(c * 64 + 8 * i) * 4096 + ch * 8, lds + slot * K1_REG + 1024 * (lw + 4 * i));
      }
#pragma unroll
      for (int i = 0; i < 3; ++i) GLDS(xsrc + (size_t)i * 32 * 448 + c * 64, lds + slot * K1_REG + K1_XO + 1024 * (lw + 4 * i));
    };
    auto load_kv = [&](const unsigned short* base, int region, bool isv) {
#pragma unroll
      for (int i = 0; i < 12; ++i) {
        int j = lw + 4 * i;
        int row = 2 * j + (lane >> 5);
        int bb = row >= 48 ? 1 : 0;
        int ch = (lane & 31) ^ (isv ? ((row & 7) << 1) : (row & 15));
        GLDS(base + (size_t)bb * 8 * 48 * 256 + (row - 48 * bb) * 256 + ch * 8, lds + region * K1_REG + 1024 * j);
      }
    };
    load_chunk(0, 0);
    load_chunk(1, 1);
#define K1_LITER(i, N)                                  \
  {                                                     \
    waitv<N>();                                         \
    BARRIER();                                          \
    if (i + 2 <= 6) load_chunk(i + 2, (i + 2) % 3);     \
    if (i == 5) load_kv(kb, 1, false);                  \
    if (i == 6) load_kv(vb, 2, true);                   \
  }
    K1_LITER(0, 11) K1_LITER(1, 11) K1_LITER(2, 11) K1_LITER(3, 11) K1_LITER(4, 11) K1_LITER(5, 11) K1_LITER(6, 12)
#undef K1_LITER
  } else {
    int a_off[2];
#pragma unroll
    for (int t = 0; t < 2; ++t) {
      int row = 8 * g + 4 * t + tq;
      int f = (row & 3) | (((row >> 3) & 1) << 2);
      int c2 = 8 * w + (tp >> 1);
      a_off[t] = row * 512 + ((c2 ^ (f << 1)) << 4) + 8 * (tp & 1);
    }
    auto compute = [&](int slot) {
      const char* wb = lds + slot * K1_REG;
#pragma unroll
      for (int s = 0; s < 2; ++s) {
        bf16x8 af[4];
#pragma unroll
        for (int dt = 0; dt < 4; ++dt) {
          bf16x4 lo = TRREAD(wb + s * 16384 + (a_off[0] ^ (dt << 5)));
          bf16x4 hi = TRREAD(wb + s * 16384 + (a_off[1] ^ (dt << 5)));
          af[dt] = __builtin_shufflevector(lo, hi, 0, 1, 2, 3, 4, 5, 6, 7);
        }
#pragma unroll
        for (int rt = 0; rt < 6; ++rt) {
          int row = 16 * rt + l15;
          bf16x8 bfr = *reinterpret_cast<const bf16x8*>(wb + K1_XO + row * 128 + (((4 * s + g) ^ (row & 7)) << 4));
#pragma unroll
          for (int dt = 0; dt < 4; ++dt) acc[dt][rt] = MFMA32(af[dt], bfr, acc[dt][rt]);
        }
      }
    };
#pragma unroll
    for (int i = 0; i < 7; ++i) {
      BARRIER();
      compute(i % 3);
    }
  }

  BARRIER();
  if (w < 4) {
#pragma unroll
    for (int dt = 0; dt < 4; ++dt)
#pragma unroll
      for (int rt = 0; rt < 6; ++rt) {
        int row = 16 * rt + l15;
        int chunk = 8 * w + 2 * dt + (g >> 1);
        u32x2 v;
        v[0] = pack2(acc[dt][rt][0], acc[dt][rt][1]);
        v[1] = pack2(acc[dt][rt][2], acc[dt][rt][3]);
        *reinterpret_cast<u32x2*>(lds + K1_Q_OFF + row * 512 + ((chunk ^ (row & 15)) << 4) + 8 * (g & 1)) = v;
      }
  }
  WAITV(0);
  __syncthreads();

  if (w < 6) {
    const int bb = w / 3, qt = w - 3 * bb;
    bf16x8 bq[8];
    {
      int row = 48 * bb + 16 * qt + l15;
#pragma unroll
      for (int s = 0; s < 8; ++s)
        bq[s] = *reinterpret_cast<const bf16x8*>(lds + K1_Q_OFF + row * 512 + (((4 * s + g) ^ (row & 15)) << 4));
    }
    f32x4 sacc[3];
#pragma unroll
    for (int kt = 0; kt < 3; ++kt) {
      sacc[kt] = f32x4{0.f, 0.f, 0.f, 0.f};
      int row = 48 * bb + 16 * kt + l15;
#pragma unroll
      for (int s = 0; s < 8; ++s) {
        bf16x8 ak = *reinterpret_cast<const bf16x8*>(lds + K1_K_OFF + row * 512 + (((4 * s + g) ^ (row & 15)) << 4));
        sacc[kt] = MFMA32(ak, bq[s], sacc[kt]);
      }
    }
    float sv[12];
    float m = -INFINITY;
#pragma unroll
    for (int kt = 0; kt < 3; ++kt)
#pragma unroll
      for (int r = 0; r < 4; ++r) {
        float v = bfround(sacc[kt][r]) * 0.0625f;
        sv[kt * 4 + r] = v;
        m = fmaxf(m, v);
      }
    m = fmaxf(m, __shfl_xor(m, 16));
    m = fmaxf(m, __shfl_xor(m, 32));
    float sum = 0.f;
#pragma unroll
    for (int i = 0; i < 12; ++i) {
      sv[i] = __expf(sv[i] - m);
      sum += sv[i];
    }
    sum += __shfl_xor(sum, 16);
    sum += __shfl_xor(sum, 32);
    float inv = 1.0f / sum;
    u32x4 p0u;
    p0u[0] = pack2(sv[0] * inv, sv[1] * inv);
    p0u[1] = pack2(sv[2] * inv, sv[3] * inv);
    p0u[2] = pack2(sv[4] * inv, sv[5] * inv);
    p0u[3] = pack2(sv[6] * inv, sv[7] * inv);
    u32x2 p1u;
    p1u[0] = pack2(sv[8] * inv, sv[9] * inv);
    p1u[1] = pack2(sv[10] * inv, sv[11] * inv);
    bf16x8 p0 = __builtin_bit_cast(bf16x8, p0u);
    bf16x4 p1 = __builtin_bit_cast(bf16x4, p1u);

    const char* vbase = lds + K1_V_OFF + (48 * bb) * 512;
    int key0 = 4 * g + tq;
    int vsw = (key0 & 7) << 1;
    unsigned short* optr = ows + ((size_t)(blk * 768 + bp * 96 + 48 * bb + 16 * qt + l15)) * 4096 + h * 256 + 4 * g;
#pragma unroll 4
    for (int dt = 0; dt < 16; ++dt) {
      int coff = (((2 * dt + (tp >> 1)) ^ vsw) << 4) + 8 * (tp & 1);
      bf16x4 a0 = TRREAD(vbase + key0 * 512 + coff);
      bf16x4 a1 = TRREAD(vbase + (key0 + 16) * 512 + coff);
      bf16x4 a2 = TRREAD(vbase + (key0 + 32) * 512 + coff);
      bf16x8 a01 = __builtin_shufflevector(a0, a1, 0, 1, 2, 3, 4, 5, 6, 7);
      f32x4 o = f32x4{0.f, 0.f, 0.f, 0.f};
      o = MFMA32(a01, p0, o);
      o = MFMA16(a2, p1, o);
      u32x2 v;
      v[0] = pack2(o[0], o[1]);
      v[1] = pack2(o[2], o[3]);
      *reinterpret_cast<u32x2*>(optr + 16 * dt) = v;
    }
  }
}

constexpr int K2_SLOT = 26624;
constexpr int K2_NSLOT = 5;
constexpr int K2_LDS = K2_SLOT * K2_NSLOT + 2048;

__global__ __launch_bounds__(704, 1) void k2_oproj(
    const unsigned short* __restrict__ ows, const unsigned short* __restrict__ woA,
    const unsigned short* __restrict__ woB, float* __restrict__ part) {
  __shared__ __attribute__((aligned(16))) char lds[K2_LDS];
  const int tid = threadIdx.x;
  const int lane = tid & 63;
  const int w = __builtin_amdgcn_readfirstlane(tid >> 6);
  const int g = lane >> 4, l15 = lane & 15, tq = (lane & 15) >> 2, tp = lane & 3;
  const int wg = blockIdx.x;
  const int xcd = wg & 7, idx = wg >> 3;
  const int blk = xcd >> 2, ks = xcd & 3;
  const int mt = idx & 7, nt = idx >> 3;
  const unsigned short* wo = blk ? woB : woA;
  const unsigned short* ob = ows + (size_t)(blk * 768 + mt * 96) * 4096 + ks * 1024;
  const unsigned short* wb = wo + (size_t)(ks * 1024) * 448 + nt * 112;

  f32x4 acc[6];
#pragma unroll
  for (int b = 0; b < 6; ++b) acc[b] = f32x4{0.f, 0.f, 0.f, 0.f};

  if (w >= 7) {
    const int lw = w - 7;
    const unsigned short* src[7];
    int dsto[7];
    size_t cstep[7];
#pragma unroll
    for (int i = 0; i < 7; ++i) {
      int j = lw + 4 * i;
      int p = 1024 * j + 16 * lane;
      int rowo = p >> 7;
      int cho = ((p >> 4) & 7) ^ (rowo & 7);
      int pw = 1024 * (j - 12) + 16 * lane;
      int roww = pw / 224;
      int chw = (pw - roww * 224) >> 4;
      bool iso = j < 12, isw = (j >= 12) && (j < 26);
      const unsigned short* so = ob + (size_t)rowo * 4096 + cho * 8;
      const unsigned short* sw = wb + (size_t)roww * 448 + chw * 8;
      const unsigned short* sd = ob + (size_t)(lane >> 3) * 4096 + (lane & 7) * 8;
      src[i] = iso ? so : (isw ? sw : sd);
      cstep[i] = isw ? (size_t)64 * 448 : (size_t)64;
      dsto[i] = (iso || isw) ? 1024 * j : (K2_NSLOT * K2_SLOT + 1024 * (j - 26));
    }
    auto load_chunk = [&](int c, int slot) {
#pragma unroll
      for (int i = 0; i < 7; ++i) {
        int d = dsto[i] + ((dsto[i] < K2_NSLOT * K2_SLOT) ? slot * K2_SLOT : 0);
        GLDS(src[i] + cstep[i] * c, lds + d);
      }
    };
    load_chunk(0, 0);
    load_chunk(1, 1);
    load_chunk(2, 2);
    load_chunk(3, 3);
#define K2_ITER(i, N)                                          \
  {                                                            \
    WAITV(N);                                                  \
    BARRIER();                                                 \
    if (i + 4 <= 15) load_chunk(i + 4, (i + 4) % K2_NSLOT);    \
  }
    K2_ITER(0, 21) K2_ITER(1, 21) K2_ITER(2, 21) K2_ITER(3, 21) K2_ITER(4, 21) K2_ITER(5, 21)
    K2_ITER(6, 21) K2_ITER(7, 21) K2_ITER(8, 21) K2_ITER(9, 21) K2_ITER(10, 21) K2_ITER(11, 21)
    K2_ITER(12, 21) K2_ITER(13, 14) K2_ITER(14, 7) K2_ITER(15, 0)
#undef K2_ITER
    return;
  }

  auto compute = [&](int slot) {
    const char* ol = lds + slot * K2_SLOT;
    const char* wl = ol + 12288;
#pragma unroll
    for (int s = 0; s < 2; ++s) {
      int r0 = 32 * s + 8 * g + tq;
      bf16x4 lo = TRREAD(wl + r0 * 224 + 32 * w + 8 * tp);
      bf16x4 hi = TRREAD(wl + (r0 + 4) * 224 + 32 * w + 8 * tp);
      bf16x8 af = __builtin_shufflevector(lo, hi, 0, 1, 2, 3, 4, 5, 6, 7);
#pragma unroll
      for (int rt = 0; rt < 6; ++rt) {
        int row = 16 * rt + l15;
        bf16x8 bfr = *reinterpret_cast<const bf16x8*>(ol + row * 128 + (((4 * s + g) ^ (row & 7)) << 4));
        acc[rt] = MFMA32(af, bfr, acc[rt]);
      }
    }
  };
#pragma unroll
  for (int i = 0; i < 16; ++i) {
    BARRIER();
    compute(i % K2_NSLOT);
  }

  float* pp = part + ((size_t)((blk * 4 + ks) * 768 + mt * 96 + l15)) * 448 + nt * 112 + 16 * w + 4 * g;
#pragma unroll
  for (int rt = 0; rt < 6; ++rt) *reinterpret_cast<f32x4*>(pp + (size_t)rt * 16 * 448) = acc[rt];
}

__global__ __launch_bounds__(256) void k3_reduce(const float* __restrict__ part, unsigned short* __restrict__ out) {
  int i = blockIdx.x * 256 + threadIdx.x;
  if (i >= 2 * 768 * 112) return;
  int q = i % 112;
  int row = (i / 112) % 768;
  int blk = i / (112 * 768);
  const float* p = part + ((size_t)(blk * 4) * 768 + row) * 448 + q * 4;
  f32x4 s = *reinterpret_cast<const f32x4*>(p);
#pragma unroll
  for (int k = 1; k < 4; ++k) s += *reinterpret_cast<const f32x4*>(p + (size_t)k * 768 * 448);
  u32x2 v;
  v[0] = pack2(s[0], s[1]);
  v[1] = pack2(s[2], s[3]);
  *reinterpret_cast<u32x2*>(out + (size_t)row * 896 + blk * 448 + q * 4) = v;
}

extern "C" void kernel_launch(void* const* d_in, const int* in_sizes, int n_in, void* d_out, int out_size,
                              void* d_ws, size_t ws_size, hipStream_t stream) {
  const unsigned short* xA = (const unsigned short*)d_in[0];
  const unsigned short* kA = (const unsigned short*)d_in[1];
  const unsigned short* vA = (const unsigned short*)d_in[2];
  const unsigned short* wqA = (const unsigned short*)d_in[3];
  const unsigned short* woA = (const unsigned short*)d_in[4];
  const unsigned short* xB = (const unsigned short*)d_in[5];
  const unsigned short* kB = (const unsigned short*)d_in[6];
  const unsigned short* vB = (const unsigned short*)d_in[7];
  const unsigned short* wqB = (const unsigned short*)d_in[8];
  const unsigned short* woB = (const unsigned short*)d_in[9];
  unsigned short* ows = (unsigned short*)d_ws;
  float* part = (float*)((char*)d_ws + (size_t)2 * 768 * 4096 * 2);
  k1_qproj_attn<<<256, 512, 0, stream>>>(xA, kA, vA, wqA, xB, kB, vB, wqB, ows);
  k2_oproj<<<256, 704, 0, stream>>>(ows, woA, woB, part);
  k3_reduce<<<(2 * 768 * 112 + 255) / 256, 256, 0, stream>>>(part, (unsigned short*)d_out);
}
```

```cpp
#include <hip/hip_runtime.h>
#include <hip/hip_bf16.h>
#include <stdint.h>


typedef __attribute__((ext_vector_type(8))) short bf16x8;
typedef __attribute__((ext_vector_type(4))) short bf16x4;
typedef __attribute__((ext_vector_type(4))) float f32x4;
typedef __attribute__((ext_vector_type(4))) unsigned u32x4;
typedef __attribute__((ext_vector_type(2))) unsigned u32x2;

__device__ __forceinline__ void glds16(const void* gsrc, const void* ldsptr) {
  unsigned keep;
  unsigned dst = __builtin_amdgcn_readfirstlane((unsigned)(size_t)(__attribute__((address_space(3))) const char*)ldsptr);
  asm volatile("s_mov_b32 %0, m0\n\ts_mov_b32 m0, %2\n\ts_nop 0\n\tglobal_load_lds_dwordx4 %1, off\n\ts_mov_b32 m0, %0"
               : "=&s"(keep) : "v"(gsrc), "s"(dst) : "memory");
}
#define GLDS(gptr, ldsptr) glds16((const void*)(gptr), (const void*)(ldsptr))
#define TRREAD(ldsptr) \
  __builtin_amdgcn_ds_read_tr16_b64_v4i16((__attribute__((address_space(3))) bf16x4*)(ldsptr))
#define MFMA32(a, b, c) __builtin_amdgcn_mfma_f32_16x16x32_bf16((a), (b), (c), 0, 0, 0)
#define MFMA16(a, b, c) __builtin_amdgcn_mfma_f32_16x16x16bf16_1k((a), (b), (c), 0, 0, 0)
#define WAITV(n) asm volatile("s_waitcnt vmcnt(" #n ")" ::: "memory")
#define BARRIER()                     \
  do {                                \
    asm volatile("" ::: "memory");    \
    __builtin_amdgcn_s_barrier();     \
    asm volatile("" ::: "memory");    \
  } while (0)

__device__ __forceinline__ unsigned short f2bf(float f) {
  __hip_bfloat16 h = __float2bfloat16(f);
  return *reinterpret_cast<unsigned short*>(&h);
}
__device__ __forceinline__ float bfround(float f) {
  return __bfloat162float(__float2bfloat16(f));
}
__device__ __forceinline__ unsigned pack2(float a, float b) {
  return (unsigned)f2bf(a) | ((unsigned)f2bf(b) << 16);
}

constexpr int K1_REG = 49152;
constexpr int K1_XO = 32768;
constexpr int K1_Q_OFF = 0;
constexpr int K1_K_OFF = K1_REG;
constexpr int K1_V_OFF = 2 * K1_REG;
constexpr int K1_LDS = 3 * K1_REG;

template <int WAITN>
__device__ __forceinline__ void waitv() {
  if constexpr (WAITN == 12) WAITV(12);
  else if constexpr (WAITN == 11) WAITV(11);
  else if constexpr (WAITN == 8) WAITV(8);
  else if constexpr (WAITN == 7) WAITV(7);
  else if constexpr (WAITN == 6) WAITV(6);
  else if constexpr (WAITN == 5) WAITV(5);
  else if constexpr (WAITN == 4) WAITV(4);
  else WAITV(0);
}

__global__ __launch_bounds__(512, 1) void k1_qproj_attn(
    const unsigned short* __restrict__ xA, const unsigned short* __restrict__ kA,
    const unsigned short* __restrict__ vA, const unsigned short* __restrict__ wqA,
    const unsigned short* __restrict__ xB, const unsigned short* __restrict__ kB,
    const unsigned short* __restrict__ vB, const unsigned short* __restrict__ wqB,
    unsigned short* __restrict__ ows, const unsigned short* __restrict__ woA,
    const unsigned short* __restrict__ woB) {
  __shared__ __attribute__((aligned(16))) char lds[K1_LDS];
  const int tid = threadIdx.x;
  const int lane = tid & 63;
  const int w = __builtin_amdgcn_readfirstlane(tid >> 6);
  const int g = lane >> 4, l15 = lane & 15, tq = (lane & 15) >> 2, tp = lane & 3;
  const int wg = blockIdx.x;
  const int xcd = wg & 7, idx = wg >> 3;
  const int blk = xcd >> 2;
  const int h = 4 * (xcd & 3) + (idx & 3);
  const int bp = idx >> 2;
  const unsigned short* x = blk ? xB : xA;
  const unsigned short* kc = blk ? kB : kA;
  const unsigned short* vc = blk ? vB : vA;
  const unsigned short* wq = blk ? wqB : wqA;
  const int kvh = h >> 1;

  const int lw = w - 4;
  const unsigned short* kb = kc + ((size_t)(bp * 2) * 8 + kvh) * 48 * 256;
  const unsigned short* vb = vc + ((size_t)(bp * 2) * 8 + kvh) * 48 * 256;
  f32x4 acc[4][6];
#pragma unroll
  for (int a = 0; a < 4; ++a)
#pragma unroll
    for (int b = 0; b < 6; ++b) acc[a][b] = f32x4{0.f, 0.f, 0.f, 0.f};

  if (w >= 4) {
    const unsigned short* wsrc;
    const unsigned short* xsrc;
    {
      int row = 2 * lw + (lane >> 5);
      int f = (row & 3);
      wsrc = wq + h * 256 + (size_t)row * 4096;
      (void)f;
      int p = 1024 * lw + 16 * lane;
      int xr = p >> 7;
      xsrc = x + (size_t)bp * 96 * 448 + xr * 448 + ((((p >> 4) & 7) ^ (xr & 7)) << 3);
    }
    auto load_chunk = [&](int c, int slot) {
#pragma unroll
      for (int i = 0; i < 8; ++i) {
        int row = 2 * (lw + 4 * i) + (lane >> 5);
        int f = (row & 3) | (((row >> 3) & 1) << 2);
        int ch = (lane & 31) ^ (f << 1);
        GLDS(wsrc + (size_t)(c * 64 + 8 * i) * 4096 + ch * 8, lds + slot * K1_REG + 1024 * (lw + 4 * i));
      }
#pragma unroll
      for (int i = 0; i < 3; ++i) GLDS(xsrc + (size_t)i * 32 * 448 + c * 64, lds + slot * K1_REG + K1_XO + 1024 * (lw + 4 * i));
    };
    auto load_kv = [&](const unsigned short* base, int region, bool isv) {
#pragma unroll
      for (int i = 0; i < 12; ++i) {
        int j = lw + 4 * i;
        int row = 2 * j + (lane >> 5);
        int bb = row >= 48 ? 1 : 0;
        int ch = (lane & 31) ^ (isv ? ((row & 7) << 1) : (row & 15));
        GLDS(base + (size_t)bb * 8 * 48 * 256 + (row - 48 * bb) * 256 + ch * 8, lds + region * K1_REG + 1024 * j);
      }
    };
    load_chunk(0, 0);
    load_chunk(1, 1);
    unsigned wdummy;
    {
      int ln = lw * 64 + lane;
      ln = ln < 224 ? ln : 223;
      int r = ln / 7;
      const unsigned short* pw = (blk ? woB : woA) + (size_t)((xcd & 3) * 1024 + idx * 32 + r) * 448 + (ln - 7 * r) * 64;
      asm volatile("global_load_dword %0, %1, off" : "=v"(wdummy) : "v"(pw) : "memory");
    }
#define K1_LITER(i, N)                                  \
  {                                                     \
    waitv<N>();                                         \
    BARRIER();                                          \
    if (i + 2 <= 6) load_chunk(i + 2, (i + 2) % 3);     \
    if (i == 5) load_kv(kb, 1, false);                  \
    if (i == 6) load_kv(vb, 2, true);                   \
  }
    K1_LITER(0, 12) K1_LITER(1, 12) K1_LITER(2, 11) K1_LITER(3, 11) K1_LITER(4, 11) K1_LITER(5, 11) K1_LITER(6, 12)
#undef K1_LITER
    asm volatile("" ::"v"(wdummy));
  } else {
    int a_off[2];
#pragma unroll
    for (int t = 0; t < 2; ++t) {
      int row = 8 * g + 4 * t + tq;
      int f = (row & 3) | (((row >> 3) & 1) << 2);
      int c2 = 8 * w + (tp >> 1);
      a_off[t] = row * 512 + ((c2 ^ (f << 1)) << 4) + 8 * (tp & 1);
    }
    auto compute = [&](int slot) {
      const char* wb = lds + slot * K1_REG;
#pragma unroll
      for (int s = 0; s < 2; ++s) {
        bf16x8 af[4];
#pragma unroll
        for (int dt = 0; dt < 4; ++dt) {
          bf16x4 lo = TRREAD(wb + s * 16384 + (a_off[0] ^ (dt << 5)));
          bf16x4 hi = TRREAD(wb + s * 16384 + (a_off[1] ^ (dt << 5)));
          af[dt] = __builtin_shufflevector(lo, hi, 0, 1, 2, 3, 4, 5, 6, 7);
        }
#pragma unroll
        for (int rt = 0; rt < 6; ++rt) {
          int row = 16 * rt + l15;
          bf16x8 bfr = *reinterpret_cast<const bf16x8*>(wb + K1_XO + row * 128 + (((4 * s + g) ^ (row & 7)) << 4));
#pragma unroll
          for (int dt = 0; dt < 4; ++dt) acc[dt][rt] = MFMA32(af[dt], bfr, acc[dt][rt]);
        }
      }
    };
#pragma unroll
    for (int i = 0; i < 7; ++i) {
      BARRIER();
      compute(i % 3);
    }
  }

  BARRIER();
  if (w < 4) {
#pragma unroll
    for (int dt = 0; dt < 4; ++dt)
#pragma unroll
      for (int rt = 0; rt < 6; ++rt) {
        int row = 16 * rt + l15;
        int chunk = 8 * w + 2 * dt + (g >> 1);
        u32x2 v;
        v[0] = pack2(acc[dt][rt][0], acc[dt][rt][1]);
        v[1] = pack2(acc[dt][rt][2], acc[dt][rt][3]);
        *reinterpret_cast<u32x2*>(lds + K1_Q_OFF + row * 512 + ((chunk ^ (row & 15)) << 4) + 8 * (g & 1)) = v;
      }
  }
  WAITV(0);
  __syncthreads();

  if (w < 6) {
    const int bb = w / 3, qt = w - 3 * bb;
    bf16x8 bq[8];
    {
      int row = 48 * bb + 16 * qt + l15;
#pragma unroll
      for (int s = 0; s < 8; ++s)
        bq[s] = *reinterpret_cast<const bf16x8*>(lds + K1_Q_OFF + row * 512 + (((4 * s + g) ^ (row & 15)) << 4));
    }
    f32x4 sacc[3];
#pragma unroll
    for (int kt = 0; kt < 3; ++kt) {
      sacc[kt] = f32x4{0.f, 0.f, 0.f, 0.f};
      int row = 48 * bb + 16 * kt + l15;
#pragma unroll
      for (int s = 0; s < 8; ++s) {
        bf16x8 ak = *reinterpret_cast<const bf16x8*>(lds + K1_K_OFF + row * 512 + (((4 * s + g) ^ (row & 15)) << 4));
        sacc[kt] = MFMA32(ak, bq[s], sacc[kt]);
      }
    }
    float sv[12];
    float m = -INFINITY;
#pragma unroll
    for (int kt = 0; kt < 3; ++kt)
#pragma unroll
      for (int r = 0; r < 4; ++r) {
        float v = bfround(sacc[kt][r]) * 0.0625f;
        sv[kt * 4 + r] = v;
        m = fmaxf(m, v);
      }
    m = fmaxf(m, __shfl_xor(m, 16));
    m = fmaxf(m, __shfl_xor(m, 32));
    float sum = 0.f;
#pragma unroll
    for (int i = 0; i < 12; ++i) {
      sv[i] = __expf(sv[i] - m);
      sum += sv[i];
    }
    sum += __shfl_xor(sum, 16);
    sum += __shfl_xor(sum, 32);
    float inv = 1.0f / sum;
    u32x4 p0u;
    p0u[0] = pack2(sv[0] * inv, sv[1] * inv);
    p0u[1] = pack2(sv[2] * inv, sv[3] * inv);
    p0u[2] = pack2(sv[4] * inv, sv[5] * inv);
    p0u[3] = pack2(sv[6] * inv, sv[7] * inv);
    u32x2 p1u;
    p1u[0] = pack2(sv[8] * inv, sv[9] * inv);
    p1u[1] = pack2(sv[10] * inv, sv[11] * inv);
    bf16x8 p0 = __builtin_bit_cast(bf16x8, p0u);
    bf16x4 p1 = __builtin_bit_cast(bf16x4, p1u);

    const char* vbase = lds + K1_V_OFF + (48 * bb) * 512;
    int key0 = 4 * g + tq;
    int vsw = (key0 & 7) << 1;
    unsigned short* optr = ows + ((size_t)(blk * 768 + bp * 96 + 48 * bb + 16 * qt + l15)) * 4096 + h * 256 + 4 * g;
#pragma unroll 4
    for (int dt = 0; dt < 16; ++dt) {
      int coff = (((2 * dt + (tp >> 1)) ^ vsw) << 4) + 8 * (tp & 1);
      bf16x4 a0 = TRREAD(vbase + key0 * 512 + coff);
      bf16x4 a1 = TRREAD(vbase + (key0 + 16) * 512 + coff);
      bf16x4 a2 = TRREAD(vbase + (key0 + 32) * 512 + coff);
      bf16x8 a01 = __builtin_shufflevector(a0, a1, 0, 1, 2, 3, 4, 5, 6, 7);
      f32x4 o = f32x4{0.f, 0.f, 0.f, 0.f};
      o = MFMA32(a01, p0, o);
      o = MFMA16(a2, p1, o);
      u32x2 v;
      v[0] = pack2(o[0], o[1]);
      v[1] = pack2(o[2], o[3]);
      *reinterpret_cast<u32x2*>(optr + 16 * dt) = v;
    }
  }
}

constexpr int K2_SLOT = 26624;
constexpr int K2_NSLOT = 5;
constexpr int K2_LDS = K2_SLOT * K2_NSLOT + 2048;

__global__ __launch_bounds__(704, 1) void k2_oproj(
    const unsigned short* __restrict__ ows, const unsigned short* __restrict__ woA,
    const unsigned short* __restrict__ woB, float* __restrict__ part) {
  __shared__ __attribute__((aligned(16))) char lds[K2_LDS];
  const int tid = threadIdx.x;
  const int lane = tid & 63;
  const int w = __builtin_amdgcn_readfirstlane(tid >> 6);
  const int g = lane >> 4, l15 = lane & 15, tq = (lane & 15) >> 2, tp = lane & 3;
  const int wg = blockIdx.x;
  const int xcd = wg & 7, idx = wg >> 3;
  const int blk = xcd >> 2, ks = xcd & 3;
  const int mt = idx & 7, nt = idx >> 3;
  const unsigned short* wo = blk ? woB : woA;
  const unsigned short* ob = ows + (size_t)(blk * 768 + mt * 96) * 4096 + ks * 1024;
  const unsigned short* wb = wo + (size_t)(ks * 1024) * 448 + nt * 112;

  f32x4 acc[6];
#pragma unroll
  for (int b = 0; b < 6; ++b) acc[b] = f32x4{0.f, 0.f, 0.f, 0.f};

  if (w >= 7) {
    const int lw = w - 7;
    const unsigned short* src[7];
    int dsto[7];
    size_t cstep[7];
#pragma unroll
    for (int i = 0; i < 7; ++i) {
      int j = lw + 4 * i;
      int p = 1024 * j + 16 * lane;
      int rowo = p >> 7;
      int cho = ((p >> 4) & 7) ^ (rowo & 7);
      int pw = 1024 * (j - 12) + 16 * lane;
      int roww = pw / 224;
      int chw = (pw - roww * 224) >> 4;
      bool iso = j < 12, isw = (j >= 12) && (j < 26);
      const unsigned short* so = ob + (size_t)rowo * 4096 + cho * 8;
      const unsigned short* sw = wb + (size_t)roww * 448 + chw * 8;
      const unsigned short* sd = ob + (size_t)(lane >> 3) * 4096 + (lane & 7) * 8;
      src[i] = iso ? so : (isw ? sw : sd);
      cstep[i] = isw ? (size_t)64 * 448 : (size_t)64;
      dsto[i] = (iso || isw) ? 1024 * j : (K2_NSLOT * K2_SLOT + 1024 * (j - 26));
    }
    auto load_chunk = [&](int c, int slot) {
#pragma unroll
      for (int i = 0; i < 7; ++i) {
        int d = dsto[i] + ((dsto[i] < K2_NSLOT * K2_SLOT) ? slot * K2_SLOT : 0);
        GLDS(src[i] + cstep[i] * c, lds + d);
      }
    };
    load_chunk(0, 0);
    load_chunk(1, 1);
    load_chunk(2, 2);
    load_chunk(3, 3);
#define K2_ITER(i, N)                                          \
  {                                                            \
    WAITV(N);                                                  \
    BARRIER();                                                 \
    if (i + 4 <= 15) load_chunk(i + 4, (i + 4) % K2_NSLOT);    \
  }
    K2_ITER(0, 21) K2_ITER(1, 21) K2_ITER(2, 21) K2_ITER(3, 21) K2_ITER(4, 21) K2_ITER(5, 21)
    K2_ITER(6, 21) K2_ITER(7, 21) K2_ITER(8, 21) K2_ITER(9, 21) K2_ITER(10, 21) K2_ITER(11, 21)
    K2_ITER(12, 21) K2_ITER(13, 14) K2_ITER(14, 7) K2_ITER(15, 0)
#undef K2_ITER
    return;
  }

  auto compute = [&](int slot) {
    const char* ol = lds + slot * K2_SLOT;
    const char* wl = ol + 12288;
#pragma unroll
    for (int s = 0; s < 2; ++s) {
      int r0 = 32 * s + 8 * g + tq;
      bf16x4 lo = TRREAD(wl + r0 * 224 + 32 * w + 8 * tp);
      bf16x4 hi = TRREAD(wl + (r0 + 4) * 224 + 32 * w + 8 * tp);
      bf16x8 af = __builtin_shufflevector(lo, hi, 0, 1, 2, 3, 4, 5, 6, 7);
#pragma unroll
      for (int rt = 0; rt < 6; ++rt) {
        int row = 16 * rt + l15;
        bf16x8 bfr = *reinterpret_cast<const bf16x8*>(ol + row * 128 + (((4 * s + g) ^ (row & 7)) << 4));
        acc[rt] = MFMA32(af, bfr, acc[rt]);
      }
    }
  };
#pragma unroll
  for (int i = 0; i < 16; ++i) {
    BARRIER();
    compute(i % K2_NSLOT);
  }

  float* pp = part + ((size_t)((blk * 4 + ks) * 768 + mt * 96 + l15)) * 448 + nt * 112 + 16 * w + 4 * g;
#pragma unroll
  for (int rt = 0; rt < 6; ++rt) *reinterpret_cast<f32x4*>(pp + (size_t)rt * 16 * 448) = acc[rt];
}

__global__ __launch_bounds__(256) void k3_reduce(const float* __restrict__ part, unsigned short* __restrict__ out) {
  int i = blockIdx.x * 256 + threadIdx.x;
  if (i >= 2 * 768 * 112) return;
  int q = i % 112;
  int row = (i / 112) % 768;
  int blk = i / (112 * 768);
  const float* p = part + ((size_t)(blk * 4) * 768 + row) * 448 + q * 4;
  f32x4 s = *reinterpret_cast<const f32x4*>(p);
#pragma unroll
  for (int k = 1; k < 4; ++k) s += *reinterpret_cast<const f32x4*>(p + (size_t)k * 768 * 448);
  u32x2 v;
  v[0] = pack2(s[0], s[1]);
  v[1] = pack2(s[2], s[3]);
  *reinterpret_cast<u32x2*>(out + (size_t)row * 896 + blk * 448 + q * 4) = v;
}

extern "C" void kernel_launch(void* const* d_in, const int* in_sizes, int n_in, void* d_out, int out_size,
                              void* d_ws, size_t ws_size, hipStream_t stream) {
  const unsigned short* xA = (const unsigned short*)d_in[0];
  const unsigned short* kA = (const unsigned short*)d_in[1];
  const unsigned short* vA = (const unsigned short*)d_in[2];
  const unsigned short* wqA = (const unsigned short*)d_in[3];
  const unsigned short* woA = (const unsigned short*)d_in[4];
  const unsigned short* xB = (const unsigned short*)d_in[5];
  const unsigned short* kB = (const unsigned short*)d_in[6];
  const unsigned short* vB = (const unsigned short*)d_in[7];
  const unsigned short* wqB = (const unsigned short*)d_in[8];
  const unsigned short* woB = (const unsigned short*)d_in[9];
  unsigned short* ows = (unsigned short*)d_ws;
  float* part = (float*)((char*)d_ws + (size_t)2 * 768 * 4096 * 2);
  k1_qproj_attn<<<256, 512, 0, stream>>>(xA, kA, vA, wqA, xB, kB, vB, wqB, ows, woA, woB);
  k2_oproj<<<256, 704, 0, stream>>>(ows, woA, woB, part);
  k3_reduce<<<(2 * 768 * 112 + 255) / 256, 256, 0, stream>>>(part, (unsigned short*)d_out);
}
```

```cpp
#include <hip/hip_runtime.h>
#include <hip/hip_bf16.h>
#include <stdint.h>


typedef __attribute__((ext_vector_type(8))) short bf16x8;
typedef __attribute__((ext_vector_type(4))) short bf16x4;
typedef __attribute__((ext_vector_type(4))) float f32x4;
typedef __attribute__((ext_vector_type(4))) unsigned u32x4;
typedef __attribute__((ext_vector_type(2))) unsigned u32x2;

__device__ __forceinline__ void glds16(const void* gsrc, const void* ldsptr) {
  unsigned keep;
  unsigned dst = __builtin_amdgcn_readfirstlane((unsigned)(size_t)(__attribute__((address_space(3))) const char*)ldsptr);
  asm volatile("s_mov_b32 %0, m0\n\ts_mov_b32 m0, %2\n\ts_nop 0\n\tglobal_load_lds_dwordx4 %1, off\n\ts_mov_b32 m0, %0"
               : "=&s"(keep) : "v"(gsrc), "s"(dst) : "memory");
}
#define GLDS(gptr, ldsptr) glds16((const void*)(gptr), (const void*)(ldsptr))
#define TRREAD(ldsptr) \
  __builtin_amdgcn_ds_read_tr16_b64_v4i16((__attribute__((address_space(3))) bf16x4*)(ldsptr))
#define MFMA32(a, b, c) __builtin_amdgcn_mfma_f32_16x16x32_bf16((a), (b), (c), 0, 0, 0)
#define MFMA16(a, b, c) __builtin_amdgcn_mfma_f32_16x16x16bf16_1k((a), (b), (c), 0, 0, 0)
#define WAITV(n) asm volatile("s_waitcnt vmcnt(" #n ")" ::: "memory")
#define BARRIER()                     \
  do {                                \
    asm volatile("" ::: "memory");    \
    __builtin_amdgcn_s_barrier();     \
    asm volatile("" ::: "memory");    \
  } while (0)

__device__ __forceinline__ unsigned short f2bf(float f) {
  __hip_bfloat16 h = __float2bfloat16(f);
  return *reinterpret_cast<unsigned short*>(&h);
}
__device__ __forceinline__ float bfround(float f) {
  return __bfloat162float(__float2bfloat16(f));
}
__device__ __forceinline__ unsigned pack2(float a, float b) {
  return (unsigned)f2bf(a) | ((unsigned)f2bf(b) << 16);
}

constexpr int K1_REG = 49152;
constexpr int K1_XO = 32768;
constexpr int K1_Q_OFF = 0;
constexpr int K1_K_OFF = K1_REG;
constexpr int K1_V_OFF = 2 * K1_REG;
constexpr int K1_LDS = 3 * K1_REG;

template <int WAITN>
__device__ __forceinline__ void waitv() {
  if constexpr (WAITN == 12) WAITV(12);
  else if constexpr (WAITN == 15) WAITV(15);
  else if constexpr (WAITN == 11) WAITV(11);
  else if constexpr (WAITN == 8) WAITV(8);
  else if constexpr (WAITN == 7) WAITV(7);
  else if constexpr (WAITN == 6) WAITV(6);
  else if constexpr (WAITN == 5) WAITV(5);
  else if constexpr (WAITN == 4) WAITV(4);
  else WAITV(0);
}

__global__ __launch_bounds__(512, 1) void k1_qproj_attn(
    const unsigned short* __restrict__ xA, const unsigned short* __restrict__ kA,
    const unsigned short* __restrict__ vA, const unsigned short* __restrict__ wqA,
    const unsigned short* __restrict__ xB, const unsigned short* __restrict__ kB,
    const unsigned short* __restrict__ vB, const unsigned short* __restrict__ wqB,
    unsigned short* __restrict__ ows, const unsigned short* __restrict__ woA,
    const unsigned short* __restrict__ woB) {
  __shared__ __attribute__((aligned(16))) char lds[K1_LDS];
  const int tid = threadIdx.x;
  const int lane = tid & 63;
  const int w = __builtin_amdgcn_readfirstlane(tid >> 6);
  const int g = lane >> 4, l15 = lane & 15, tq = (lane & 15) >> 2, tp = lane & 3;
  const int wg = blockIdx.x;
  const int xcd = wg & 7, idx = wg >> 3;
  const int blk = xcd >> 2;
  const int h = 4 * (xcd & 3) + (idx & 3);
  const int bp = idx >> 2;
  const unsigned short* x = blk ? xB : xA;
  const unsigned short* kc = blk ? kB : kA;
  const unsigned short* vc = blk ? vB : vA;
  const unsigned short* wq = blk ? wqB : wqA;
  const int kvh = h >> 1;

  const int lw = w - 4;
  const unsigned short* kb = kc + ((size_t)(bp * 2) * 8 + kvh) * 48 * 256;
  const unsigned short* vb = vc + ((size_t)(bp * 2) * 8 + kvh) * 48 * 256;
  f32x4 acc[4][6];
#pragma unroll
  for (int a = 0; a < 4; ++a)
#pragma unroll
    for (int b = 0; b < 6; ++b) acc[a][b] = f32x4{0.f, 0.f, 0.f, 0.f};

  if (w >= 4) {
    const unsigned short* wsrc;
    const unsigned short* xsrc;
    {
      int row = 2 * lw + (lane >> 5);
      int f = (row & 3);
      wsrc = wq + h * 256 + (size_t)row * 4096;
      (void)f;
      int p = 1024 * lw + 16 * lane;
      int xr = p >> 7;
      xsrc = x + (size_t)bp * 96 * 448 + xr * 448 + ((((p >> 4) & 7) ^ (xr & 7)) << 3);
    }
    auto load_chunk = [&](int c, int slot) {
#pragma unroll
      for (int i = 0; i < 8; ++i) {
        int row = 2 * (lw + 4 * i) + (lane >> 5);
        int f = (row & 3) | (((row >> 3) & 1) << 2);
        int ch = (lane & 31) ^ (f << 1);
        GLDS(wsrc + (size_t)(c * 64 + 8 * i) * 4096 + ch * 8, lds + slot * K1_REG + 1024 * (lw + 4 * i));
      }
#pragma unroll
      for (int i = 0; i < 3; ++i) GLDS(xsrc + (size_t)i * 32 * 448 + c * 64, lds + slot * K1_REG + K1_XO + 1024 * (lw + 4 * i));
    };
    auto load_kv = [&](const unsigned short* base, int region, bool isv) {
#pragma unroll
      for (int i = 0; i < 12; ++i) {
        int j = lw + 4 * i;
        int row = 2 * j + (lane >> 5);
        int bb = row >= 48 ? 1 : 0;
        int ch = (lane & 31) ^ (isv ? ((row & 7) << 1) : (row & 15));
        GLDS(base + (size_t)bb * 8 * 48 * 256 + (row - 48 * bb) * 256 + ch * 8, lds + region * K1_REG + 1024 * j);
      }
    };
    load_chunk(0, 0);
    load_chunk(1, 1);
    unsigned wdummy;
    {
      int ln = lw * 64 + lane;
      ln = ln < 224 ? ln : 223;
      int r = ln / 7;
      const unsigned short* pw = (blk ? woB : woA) + (size_t)((xcd & 3) * 1024 + idx * 32 + r) * 448 + (ln - 7 * r) * 64;
      asm volatile("global_load_dword %0, %1, off" : "=v"(wdummy) : "v"(pw) : "memory");
    }
    unsigned kvdummy[3];
#pragma unroll
    for (int i = 0; i < 3; ++i) {
      int ln = (lw * 3 + i) * 64 + lane;
      int isv = ln >= 384 ? 1 : 0;
      int r = ln - 384 * isv;
      int bb = r >= 192 ? 1 : 0;
      r -= 192 * bb;
      const unsigned short* pa = (isv ? vb : kb) + (size_t)bb * 8 * 48 * 256 + r * 64;
      asm volatile("global_load_dword %0, %1, off" : "=v"(kvdummy[i]) : "v"(pa) : "memory");
    }
#define K1_LITER(i, N)                                  \
  {                                                     \
    waitv<N>();                                         \
    BARRIER();                                          \
    if (i + 2 <= 6) load_chunk(i + 2, (i + 2) % 3);     \
    if (i == 5) load_kv(kb, 1, false);                  \
    if (i == 6) load_kv(vb, 2, true);                   \
  }
    K1_LITER(0, 15) K1_LITER(1, 15) K1_LITER(2, 11) K1_LITER(3, 11) K1_LITER(4, 11) K1_LITER(5, 11) K1_LITER(6, 12)
#undef K1_LITER
    asm volatile("" ::"v"(wdummy), "v"(kvdummy[0]), "v"(kvdummy[1]), "v"(kvdummy[2]));
  } else {
    int a_off[2];
#pragma unroll
    for (int t = 0; t < 2; ++t) {
      int row = 8 * g + 4 * t + tq;
      int f = (row & 3) | (((row >> 3) & 1) << 2);
      int c2 = 8 * w + (tp >> 1);
      a_off[t] = row * 512 + ((c2 ^ (f << 1)) << 4) + 8 * (tp & 1);
    }
    auto compute = [&](int slot) {
      const char* wb = lds + slot * K1_REG;
#pragma unroll
      for (int s = 0; s < 2; ++s) {
        bf16x8 af[4];
#pragma unroll
        for (int dt = 0; dt < 4; ++dt) {
          bf16x4 lo = TRREAD(wb + s * 16384 + (a_off[0] ^ (dt << 5)));
          bf16x4 hi = TRREAD(wb + s * 16384 + (a_off[1] ^ (dt << 5)));
          af[dt] = __builtin_shufflevector(lo, hi, 0, 1, 2, 3, 4, 5, 6, 7);
        }
#pragma unroll
        for (int rt = 0; rt < 6; ++rt) {
          int row = 16 * rt + l15;
          bf16x8 bfr = *reinterpret_cast<const bf16x8*>(wb + K1_XO + row * 128 + (((4 * s + g) ^ (row & 7)) << 4));
#pragma unroll
          for (int dt = 0; dt < 4; ++dt) acc[dt][rt] = MFMA32(af[dt], bfr, acc[dt][rt]);
        }
      }
    };
#pragma unroll
    for (int i = 0; i < 7; ++i) {
      BARRIER();
      compute(i % 3);
    }
  }

  BARRIER();
  if (w < 4) {
#pragma unroll
    for (int dt = 0; dt < 4; ++dt)
#pragma unroll
      for (int rt = 0; rt < 6; ++rt) {
        int row = 16 * rt + l15;
        int chunk = 8 * w + 2 * dt + (g >> 1);
        u32x2 v;
        v[0] = pack2(acc[dt][rt][0], acc[dt][rt][1]);
        v[1] = pack2(acc[dt][rt][2], acc[dt][rt][3]);
        *reinterpret_cast<u32x2*>(lds + K1_Q_OFF + row * 512 + ((chunk ^ (row & 15)) << 4) + 8 * (g & 1)) = v;
      }
  }
  WAITV(0);
  __syncthreads();

  if (w < 6) {
    const int bb = w / 3, qt = w - 3 * bb;
    bf16x8 bq[8];
    {
      int row = 48 * bb + 16 * qt + l15;
#pragma unroll
      for (int s = 0; s < 8; ++s)
        bq[s] = *reinterpret_cast<const bf16x8*>(lds + K1_Q_OFF + row * 512 + (((4 * s + g) ^ (row & 15)) << 4));
    }
    f32x4 sacc[3];
#pragma unroll
    for (int kt = 0; kt < 3; ++kt) {
      sacc[kt] = f32x4{0.f, 0.f, 0.f, 0.f};
      int row = 48 * bb + 16 * kt + l15;
#pragma unroll
      for (int s = 0; s < 8; ++s) {
        bf16x8 ak = *reinterpret_cast<const bf16x8*>(lds + K1_K_OFF + row * 512 + (((4 * s + g) ^ (row & 15)) << 4));
        sacc[kt] = MFMA32(ak, bq[s], sacc[kt]);
      }
    }
    float sv[12];
    float m = -INFINITY;
#pragma unroll
    for (int kt = 0; kt < 3; ++kt)
#pragma unroll
      for (int r = 0; r < 4; ++r) {
        float v = bfround(sacc[kt][r]) * 0.0625f;
        sv[kt * 4 + r] = v;
        m = fmaxf(m, v);
      }
    m = fmaxf(m, __shfl_xor(m, 16));
    m = fmaxf(m, __shfl_xor(m, 32));
    float sum = 0.f;
#pragma unroll
    for (int i = 0; i < 12; ++i) {
      sv[i] = __expf(sv[i] - m);
      sum += sv[i];
    }
    sum += __shfl_xor(sum, 16);
    sum += __shfl_xor(sum, 32);
    float inv = 1.0f / sum;
    u32x4 p0u;
    p0u[0] = pack2(sv[0] * inv, sv[1] * inv);
    p0u[1] = pack2(sv[2] * inv, sv[3] * inv);
    p0u[2] = pack2(sv[4] * inv, sv[5] * inv);
    p0u[3] = pack2(sv[6] * inv, sv[7] * inv);
    u32x2 p1u;
    p1u[0] = pack2(sv[8] * inv, sv[9] * inv);
    p1u[1] = pack2(sv[10] * inv, sv[11] * inv);
    bf16x8 p0 = __builtin_bit_cast(bf16x8, p0u);
    bf16x4 p1 = __builtin_bit_cast(bf16x4, p1u);

    const char* vbase = lds + K1_V_OFF + (48 * bb) * 512;
    int key0 = 4 * g + tq;
    int vsw = (key0 & 7) << 1;
    unsigned short* optr = ows + ((size_t)(blk * 768 + bp * 96 + 48 * bb + 16 * qt + l15)) * 4096 + h * 256 + 4 * g;
#pragma unroll 4
    for (int dt = 0; dt < 16; ++dt) {
      int coff = (((2 * dt + (tp >> 1)) ^ vsw) << 4) + 8 * (tp & 1);
      bf16x4 a0 = TRREAD(vbase + key0 * 512 + coff);
      bf16x4 a1 = TRREAD(vbase + (key0 + 16) * 512 + coff);
      bf16x4 a2 = TRREAD(vbase + (key0 + 32) * 512 + coff);
      bf16x8 a01 = __builtin_shufflevector(a0, a1, 0, 1, 2, 3, 4, 5, 6, 7);
      f32x4 o = f32x4{0.f, 0.f, 0.f, 0.f};
      o = MFMA32(a01, p0, o);
      o = MFMA16(a2, p1, o);
      u32x2 v;
      v[0] = pack2(o[0], o[1]);
      v[1] = pack2(o[2], o[3]);
      *reinterpret_cast<u32x2*>(optr + 16 * dt) = v;
    }
  }
}

constexpr int K2_SLOT = 26624;
constexpr int K2_NSLOT = 5;
constexpr int K2_LDS = K2_SLOT * K2_NSLOT + 2048;

__global__ __launch_bounds__(704, 1) void k2_oproj(
    const unsigned short* __restrict__ ows, const unsigned short* __restrict__ woA,
    const unsigned short* __restrict__ woB, float* __restrict__ part) {
  __shared__ __attribute__((aligned(16))) char lds[K2_LDS];
  const int tid = threadIdx.x;
  const int lane = tid & 63;
  const int w = __builtin_amdgcn_readfirstlane(tid >> 6);
  const int g = lane >> 4, l15 = lane & 15, tq = (lane & 15) >> 2, tp = lane & 3;
  const int wg = blockIdx.x;
  const int xcd = wg & 7, idx = wg >> 3;
  const int blk = xcd >> 2, ks = xcd & 3;
  const int mt = idx & 7, nt = idx >> 3;
  const unsigned short* wo = blk ? woB : woA;
  const unsigned short* ob = ows + (size_t)(blk * 768 + mt * 96) * 4096 + ks * 1024;
  const unsigned short* wb = wo + (size_t)(ks * 1024) * 448 + nt * 112;

  f32x4 acc[6];
#pragma unroll
  for (int b = 0; b < 6; ++b) acc[b] = f32x4{0.f, 0.f, 0.f, 0.f};

  if (w >= 7) {
    const int lw = w - 7;
    const unsigned short* src[7];
    int dsto[7];
    size_t cstep[7];
#pragma unroll
    for (int i = 0; i < 7; ++i) {
      int j = lw + 4 * i;
      int p = 1024 * j + 16 * lane;
      int rowo = p >> 7;
      int cho = ((p >> 4) & 7) ^ (rowo & 7);
      int pw = 1024 * (j - 12) + 16 * lane;
      int roww = pw / 224;
      int chw = (pw - roww * 224) >> 4;
      bool iso = j < 12, isw = (j >= 12) && (j < 26);
      const unsigned short* so = ob + (size_t)rowo * 4096 + cho * 8;
      const unsigned short* sw = wb + (size_t)roww * 448 + chw * 8;
      const unsigned short* sd = ob + (size_t)(lane >> 3) * 4096 + (lane & 7) * 8;
      src[i] = iso ? so : (isw ? sw : sd);
      cstep[i] = isw ? (size_t)64 * 448 : (size_t)64;
      dsto[i] = (iso || isw) ? 1024 * j : (K2_NSLOT * K2_SLOT + 1024 * (j - 26));
    }
    auto load_chunk = [&](int c, int slot) {
#pragma unroll
      for (int i = 0; i < 7; ++i) {
        int d = dsto[i] + ((dsto[i] < K2_NSLOT * K2_SLOT) ? slot * K2_SLOT : 0);
        GLDS(src[i] + cstep[i] * c, lds + d);
      }
    };
    load_chunk(0, 0);
    load_chunk(1, 1);
    load_chunk(2, 2);
    load_chunk(3, 3);
#define K2_ITER(i, N)                                          \
  {                                                            \
    WAITV(N);                                                  \
    BARRIER();                                                 \
    if (i + 4 <= 15) load_chunk(i + 4, (i + 4) % K2_NSLOT);    \
  }
    K2_ITER(0, 21) K2_ITER(1, 21) K2_ITER(2, 21) K2_ITER(3, 21) K2_ITER(4, 21) K2_ITER(5, 21)
    K2_ITER(6, 21) K2_ITER(7, 21) K2_ITER(8, 21) K2_ITER(9, 21) K2_ITER(10, 21) K2_ITER(11, 21)
    K2_ITER(12, 21) K2_ITER(13, 14) K2_ITER(14, 7) K2_ITER(15, 0)
#undef K2_ITER
    return;
  }

  auto compute = [&](int slot) {
    const char* ol = lds + slot * K2_SLOT;
    const char* wl = ol + 12288;
#pragma unroll
    for (int s = 0; s < 2; ++s) {
      int r0 = 32 * s + 8 * g + tq;
      bf16x4 lo = TRREAD(wl + r0 * 224 + 32 * w + 8 * tp);
      bf16x4 hi = TRREAD(wl + (r0 + 4) * 224 + 32 * w + 8 * tp);
      bf16x8 af = __builtin_shufflevector(lo, hi, 0, 1, 2, 3, 4, 5, 6, 7);
#pragma unroll
      for (int rt = 0; rt < 6; ++rt) {
        int row = 16 * rt + l15;
        bf16x8 bfr = *reinterpret_cast<const bf16x8*>(ol + row * 128 + (((4 * s + g) ^ (row & 7)) << 4));
        acc[rt] = MFMA32(af, bfr, acc[rt]);
      }
    }
  };
#pragma unroll
  for (int i = 0; i < 16; ++i) {
    BARRIER();
    compute(i % K2_NSLOT);
  }

  float* pp = part + ((size_t)((blk * 4 + ks) * 768 + mt * 96 + l15)) * 448 + nt * 112 + 16 * w + 4 * g;
#pragma unroll
  for (int rt = 0; rt < 6; ++rt) *reinterpret_cast<f32x4*>(pp + (size_t)rt * 16 * 448) = acc[rt];
}

__global__ __launch_bounds__(256) void k3_reduce(const float* __restrict__ part, unsigned short* __restrict__ out) {
  int i = blockIdx.x * 256 + threadIdx.x;
  if (i >= 2 * 768 * 112) return;
  int q = i % 112;
  int row = (i / 112) % 768;
  int blk = i / (112 * 768);
  const float* p = part + ((size_t)(blk * 4) * 768 + row) * 448 + q * 4;
  f32x4 s = *reinterpret_cast<const f32x4*>(p);
#pragma unroll
  for (int k = 1; k < 4; ++k) s += *reinterpret_cast<const f32x4*>(p + (size_t)k * 768 * 448);
  u32x2 v;
  v[0] = pack2(s[0], s[1]);
  v[1] = pack2(s[2], s[3]);
  *reinterpret_cast<u32x2*>(out + (size_t)row * 896 + blk * 448 + q * 4) = v;
}

extern "C" void kernel_launch(void* const* d_in, const int* in_sizes, int n_in, void* d_out, int out_size,
                              void* d_ws, size_t ws_size, hipStream_t stream) {
  const unsigned short* xA = (const unsigned short*)d_in[0];
  const unsigned short* kA = (const unsigned short*)d_in[1];
  const unsigned short* vA = (const unsigned short*)d_in[2];
  const unsigned short* wqA = (const unsigned short*)d_in[3];
  const unsigned short* woA = (const unsigned short*)d_in[4];
  const unsigned short* xB = (const unsigned short*)d_in[5];
  const unsigned short* kB = (const unsigned short*)d_in[6];
  const unsigned short* vB = (const unsigned short*)d_in[7];
  const unsigned short* wqB = (const unsigned short*)d_in[8];
  const unsigned short* woB = (const unsigned short*)d_in[9];
  unsigned short* ows = (unsigned short*)d_ws;
  float* part = (float*)((char*)d_ws + (size_t)2 * 768 * 4096 * 2);
  k1_qproj_attn<<<256, 512, 0, stream>>>(xA, kA, vA, wqA, xB, kB, vB, wqB, ows, woA, woB);
  k2_oproj<<<256, 704, 0, stream>>>(ows, woA, woB, part);
  k3_reduce<<<(2 * 768 * 112 + 255) / 256, 256, 0, stream>>>(part, (unsigned short*)d_out);
}
```

```cpp
#include <hip/hip_runtime.h>
#include <hip/hip_bf16.h>
#include <stdint.h>


typedef __attribute__((ext_vector_type(8))) short bf16x8;
typedef __attribute__((ext_vector_type(4))) short bf16x4;
typedef __attribute__((ext_vector_type(4))) float f32x4;
typedef __attribute__((ext_vector_type(4))) unsigned u32x4;
typedef __attribute__((ext_vector_type(2))) unsigned u32x2;

__device__ __forceinline__ void glds16(const void* gsrc, const void* ldsptr) {
  unsigned keep;
  unsigned dst = __builtin_amdgcn_readfirstlane((unsigned)(size_t)(__attribute__((address_space(3))) const char*)ldsptr);
  asm volatile("s_mov_b32 %0, m0\n\ts_mov_b32 m0, %2\n\ts_nop 0\n\tglobal_load_lds_dwordx4 %1, off\n\ts_mov_b32 m0, %0"
               : "=&s"(keep) : "v"(gsrc), "s"(dst) : "memory");
}
#define GLDS(gptr, ldsptr) glds16((const void*)(gptr), (const void*)(ldsptr))
#define TRREAD(ldsptr) \
  __builtin_amdgcn_ds_read_tr16_b64_v4i16((__attribute__((address_space(3))) bf16x4*)(ldsptr))
#define MFMA32(a, b, c) __builtin_amdgcn_mfma_f32_16x16x32_bf16((a), (b), (c), 0, 0, 0)
#define MFMA16(a, b, c) __builtin_amdgcn_mfma_f32_16x16x16bf16_1k((a), (b), (c), 0, 0, 0)
#define WAITV(n) asm volatile("s_waitcnt vmcnt(" #n ")" ::: "memory")
#define BARRIER()                     \
  do {                                \
    asm volatile("" ::: "memory");    \
    __builtin_amdgcn_s_barrier();     \
    asm volatile("" ::: "memory");    \
  } while (0)

__device__ __forceinline__ unsigned short f2bf(float f) {
  __hip_bfloat16 h = __float2bfloat16(f);
  return *reinterpret_cast<unsigned short*>(&h);
}
__device__ __forceinline__ float bfround(float f) {
  return __bfloat162float(__float2bfloat16(f));
}
__device__ __forceinline__ unsigned pack2(float a, float b) {
  return (unsigned)f2bf(a) | ((unsigned)f2bf(b) << 16);
}

constexpr int K1_REG = 49152;
constexpr int K1_XO = 32768;
constexpr int K1_Q_OFF = 0;
constexpr int K1_K_OFF = K1_REG;
constexpr int K1_V_OFF = 2 * K1_REG;
constexpr int K1_LDS = 3 * K1_REG;

template <int WAITN>
__device__ __forceinline__ void waitv() {
  if constexpr (WAITN == 12) WAITV(12);
  else if constexpr (WAITN == 15) WAITV(15);
  else if constexpr (WAITN == 11) WAITV(11);
  else if constexpr (WAITN == 8) WAITV(8);
  else if constexpr (WAITN == 7) WAITV(7);
  else if constexpr (WAITN == 6) WAITV(6);
  else if constexpr (WAITN == 5) WAITV(5);
  else if constexpr (WAITN == 4) WAITV(4);
  else WAITV(0);
}

__global__ __launch_bounds__(512, 1) void k1_qproj_attn(
    const unsigned short* __restrict__ xA, const unsigned short* __restrict__ kA,
    const unsigned short* __restrict__ vA, const unsigned short* __restrict__ wqA,
    const unsigned short* __restrict__ xB, const unsigned short* __restrict__ kB,
    const unsigned short* __restrict__ vB, const unsigned short* __restrict__ wqB,
    unsigned short* __restrict__ ows, const unsigned short* __restrict__ woA,
    const unsigned short* __restrict__ woB) {
  __shared__ __attribute__((aligned(16))) char lds[K1_LDS];
  const int tid = threadIdx.x;
  const int lane = tid & 63;
  const int w = __builtin_amdgcn_readfirstlane(tid >> 6);
  const int g = lane >> 4, l15 = lane & 15, tq = (lane & 15) >> 2, tp = lane & 3;
  const int wg = blockIdx.x;
  const int xcd = wg & 7, idx = wg >> 3;
  const int blk = xcd >> 2;
  const int h = 4 * (xcd & 3) + (idx & 3);
  const int bp = idx >> 2;
  const unsigned short* x = blk ? xB : xA;
  const unsigned short* kc = blk ? kB : kA;
  const unsigned short* vc = blk ? vB : vA;
  const unsigned short* wq = blk ? wqB : wqA;
  const int kvh = h >> 1;

  const int lw = w - 4;
  const unsigned short* kb = kc + ((size_t)(bp * 2) * 8 + kvh) * 48 * 256;
  const unsigned short* vb = vc + ((size_t)(bp * 2) * 8 + kvh) * 48 * 256;
  f32x4 acc[4][6];
#pragma unroll
  for (int a = 0; a < 4; ++a)
#pragma unroll
    for (int b = 0; b < 6; ++b) acc[a][b] = f32x4{0.f, 0.f, 0.f, 0.f};

  if (w >= 4) {
    const unsigned short* wsrc;
    const unsigned short* xsrc;
    {
      int row = 2 * lw + (lane >> 5);
      int f = (row & 3);
      wsrc = wq + h * 256 + (size_t)row * 4096;
      (void)f;
      int p = 1024 * lw + 16 * lane;
      int xr = p >> 7;
      xsrc = x + (size_t)bp * 96 * 448 + xr * 448 + ((((p >> 4) & 7) ^ (xr & 7)) << 3);
    }
    auto load_chunk = [&](int c, int slot) {
#pragma unroll
      for (int i = 0; i < 8; ++i) {
        int row = 2 * (lw + 4 * i) + (lane >> 5);
        int f = (row & 3) | (((row >> 3) & 1) << 2);
        int ch = (lane & 31) ^ (f << 1);
        GLDS(wsrc + (size_t)(c * 64 + 8 * i) * 4096 + ch * 8, lds + slot * K1_REG + 1024 * (lw + 4 * i));
      }
#pragma unroll
      for (int i = 0; i < 3; ++i) GLDS(xsrc + (size_t)i * 32 * 448 + c * 64, lds + slot * K1_REG + K1_XO + 1024 * (lw + 4 * i));
    };
    auto load_kv = [&](const unsigned short* base, int region, bool isv) {
#pragma unroll
      for (int i = 0; i < 12; ++i) {
        int j = lw + 4 * i;
        int row = 2 * j + (lane >> 5);
        int bb = row >= 48 ? 1 : 0;
        int ch = (lane & 31) ^ (isv ? ((row & 7) << 1) : (row & 15));
        GLDS(base + (size_t)bb * 8 * 48 * 256 + (row - 48 * bb) * 256 + ch * 8, lds + region * K1_REG + 1024 * j);
      }
    };
    load_chunk(0, 0);
    load_chunk(1, 1);
    unsigned wdummy;
    {
      int ln = lw * 64 + lane;
      ln = ln < 224 ? ln : 223;
      int r = ln / 7;
      const unsigned short* pw = (blk ? woB : woA) + (size_t)((xcd & 3) * 1024 + idx * 32 + r) * 448 + (ln - 7 * r) * 64;
      asm volatile("global_load_dword %0, %1, off" : "=v"(wdummy) : "v"(pw) : "memory");
    }
#define K1_LITER(i, N)                                  \
  {                                                     \
    waitv<N>();                                         \
    BARRIER();                                          \
    if (i + 2 <= 6) load_chunk(i + 2, (i + 2) % 3);     \
    if (i == 5) load_kv(kb, 1, false);                  \
    if (i == 6) load_kv(vb, 2, true);                   \
  }
    K1_LITER(0, 12) K1_LITER(1, 12) K1_LITER(2, 11) K1_LITER(3, 11) K1_LITER(4, 11) K1_LITER(5, 11) K1_LITER(6, 12)
#undef K1_LITER
    asm volatile("" ::"v"(wdummy));
  } else {
    int a_off[2];
#pragma unroll
    for (int t = 0; t < 2; ++t) {
      int row = 8 * g + 4 * t + tq;
      int f = (row & 3) | (((row >> 3) & 1) << 2);
      int c2 = 8 * w + (tp >> 1);
      a_off[t] = row * 512 + ((c2 ^ (f << 1)) << 4) + 8 * (tp & 1);
    }
    auto compute = [&](int slot) {
      const char* wb = lds + slot * K1_REG;
#pragma unroll
      for (int s = 0; s < 2; ++s) {
        bf16x8 af[4];
#pragma unroll
        for (int dt = 0; dt < 4; ++dt) {
          bf16x4 lo = TRREAD(wb + s * 16384 + (a_off[0] ^ (dt << 5)));
          bf16x4 hi = TRREAD(wb + s * 16384 + (a_off[1] ^ (dt << 5)));
          af[dt] = __builtin_shufflevector(lo, hi, 0, 1, 2, 3, 4, 5, 6, 7);
        }
#pragma unroll
        for (int rt = 0; rt < 6; ++rt) {
          int row = 16 * rt + l15;
          bf16x8 bfr = *reinterpret_cast<const bf16x8*>(wb + K1_XO + row * 128 + (((4 * s + g) ^ (row & 7)) << 4));
#pragma unroll
          for (int dt = 0; dt < 4; ++dt) acc[dt][rt] = MFMA32(af[dt], bfr, acc[dt][rt]);
        }
      }
    };
#pragma unroll
    for (int i = 0; i < 7; ++i) {
      BARRIER();
      compute(i % 3);
    }
  }

  BARRIER();
  if (w < 4) {
#pragma unroll
    for (int dt = 0; dt < 4; ++dt)
#pragma unroll
      for (int rt = 0; rt < 6; ++rt) {
        int row = 16 * rt + l15;
        int chunk = 8 * w + 2 * dt + (g >> 1);
        u32x2 v;
        v[0] = pack2(acc[dt][rt][0], acc[dt][rt][1]);
        v[1] = pack2(acc[dt][rt][2], acc[dt][rt][3]);
        *reinterpret_cast<u32x2*>(lds + K1_Q_OFF + row * 512 + ((chunk ^ (row & 15)) << 4) + 8 * (g & 1)) = v;
      }
  }
  WAITV(0);
  __syncthreads();

  if (w < 6) {
    const int bb = w / 3, qt = w - 3 * bb;
    bf16x8 bq[8];
    {
      int row = 48 * bb + 16 * qt + l15;
#pragma unroll
      for (int s = 0; s < 8; ++s)
        bq[s] = *reinterpret_cast<const bf16x8*>(lds + K1_Q_OFF + row * 512 + (((4 * s + g) ^ (row & 15)) << 4));
    }
    f32x4 sacc[3];
#pragma unroll
    for (int kt = 0; kt < 3; ++kt) {
      sacc[kt] = f32x4{0.f, 0.f, 0.f, 0.f};
      int row = 48 * bb + 16 * kt + l15;
#pragma unroll
      for (int s = 0; s < 8; ++s) {
        bf16x8 ak = *reinterpret_cast<const bf16x8*>(lds + K1_K_OFF + row * 512 + (((4 * s + g) ^ (row & 15)) << 4));
        sacc[kt] = MFMA32(ak, bq[s], sacc[kt]);
      }
    }
    float sv[12];
    float m = -INFINITY;
#pragma unroll
    for (int kt = 0; kt < 3; ++kt)
#pragma unroll
      for (int r = 0; r < 4; ++r) {
        float v = bfround(sacc[kt][r]) * 0.0625f;
        sv[kt * 4 + r] = v;
        m = fmaxf(m, v);
      }
    m = fmaxf(m, __shfl_xor(m, 16));
    m = fmaxf(m, __shfl_xor(m, 32));
    float sum = 0.f;
#pragma unroll
    for (int i = 0; i < 12; ++i) {
      sv[i] = __expf(sv[i] - m);
      sum += sv[i];
    }
    sum += __shfl_xor(sum, 16);
    sum += __shfl_xor(sum, 32);
    float inv = 1.0f / sum;
    u32x4 p0u;
    p0u[0] = pack2(sv[0] * inv, sv[1] * inv);
    p0u[1] = pack2(sv[2] * inv, sv[3] * inv);
    p0u[2] = pack2(sv[4] * inv, sv[5] * inv);
    p0u[3] = pack2(sv[6] * inv, sv[7] * inv);
    u32x2 p1u;
    p1u[0] = pack2(sv[8] * inv, sv[9] * inv);
    p1u[1] = pack2(sv[10] * inv, sv[11] * inv);
    bf16x8 p0 = __builtin_bit_cast(bf16x8, p0u);
    bf16x4 p1 = __builtin_bit_cast(bf16x4, p1u);

    const char* vbase = lds + K1_V_OFF + (48 * bb) * 512;
    int key0 = 4 * g + tq;
    int vsw = (key0 & 7) << 1;
    unsigned short* optr = ows + ((size_t)(blk * 768 + bp * 96 + 48 * bb + 16 * qt + l15)) * 4096 + h * 256 + 4 * g;
#pragma unroll 4
    for (int dt = 0; dt < 16; ++dt) {
      int coff = (((2 * dt + (tp >> 1)) ^ vsw) << 4) + 8 * (tp & 1);
      bf16x4 a0 = TRREAD(vbase + key0 * 512 + coff);
      bf16x4 a1 = TRREAD(vbase + (key0 + 16) * 512 + coff);
      bf16x4 a2 = TRREAD(vbase + (key0 + 32) * 512 + coff);
      bf16x8 a01 = __builtin_shufflevector(a0, a1, 0, 1, 2, 3, 4, 5, 6, 7);
      f32x4 o = f32x4{0.f, 0.f, 0.f, 0.f};
      o = MFMA32(a01, p0, o);
      o = MFMA16(a2, p1, o);
      u32x2 v;
      v[0] = pack2(o[0], o[1]);
      v[1] = pack2(o[2], o[3]);
      *reinterpret_cast<u32x2*>(optr + 16 * dt) = v;
    }
  }
}

constexpr int K2_SLOT = 26624;
constexpr int K2_NSLOT = 5;
constexpr int K2_LDS = K2_SLOT * K2_NSLOT + 2048;

__global__ __launch_bounds__(704, 1) void k2_oproj(
    const unsigned short* __restrict__ ows, const unsigned short* __restrict__ woA,
    const unsigned short* __restrict__ woB, float* __restrict__ part) {
  __shared__ __attribute__((aligned(16))) char lds[K2_LDS];
  const int tid = threadIdx.x;
  const int lane = tid & 63;
  const int w = __builtin_amdgcn_readfirstlane(tid >> 6);
  const int g = lane >> 4, l15 = lane & 15, tq = (lane & 15) >> 2, tp = lane & 3;
  const int wg = blockIdx.x;
  const int xcd = wg & 7, idx = wg >> 3;
  const int blk = xcd >> 2, ks = xcd & 3;
  const int mt = idx & 7, nt = idx >> 3;
  const unsigned short* wo = blk ? woB : woA;
  const unsigned short* ob = ows + (size_t)(blk * 768 + mt * 96) * 4096 + ks * 1024;
  const unsigned short* wb = wo + (size_t)(ks * 1024) * 448 + nt * 112;

  f32x4 acc[6];
#pragma unroll
  for (int b = 0; b < 6; ++b) acc[b] = f32x4{0.f, 0.f, 0.f, 0.f};

  if (w >= 7) {
    const int lw = w - 7;
    const unsigned short* src[7];
    int dsto[7];
    size_t cstep[7];
#pragma unroll
    for (int i = 0; i < 7; ++i) {
      int j = lw + 4 * i;
      int p = 1024 * j + 16 * lane;
      int rowo = p >> 7;
      int cho = ((p >> 4) & 7) ^ (rowo & 7);
      int pw = 1024 * (j - 12) + 16 * lane;
      int roww = pw / 224;
      int chw = (pw - roww * 224) >> 4;
      bool iso = j < 12, isw = (j >= 12) && (j < 26);
      const unsigned short* so = ob + (size_t)rowo * 4096 + cho * 8;
      const unsigned short* sw = wb + (size_t)roww * 448 + chw * 8;
      const unsigned short* sd = ob + (size_t)(lane >> 3) * 4096 + (lane & 7) * 8;
      src[i] = iso ? so : (isw ? sw : sd);
      cstep[i] = isw ? (size_t)64 * 448 : (size_t)64;
      dsto[i] = (iso || isw) ? 1024 * j : (K2_NSLOT * K2_SLOT + 1024 * (j - 26));
    }
    auto load_chunk = [&](int c, int slot) {
#pragma unroll
      for (int i = 0; i < 7; ++i) {
        int d = dsto[i] + ((dsto[i] < K2_NSLOT * K2_SLOT) ? slot * K2_SLOT : 0);
        GLDS(src[i] + cstep[i] * c, lds + d);
      }
    };
    load_chunk(0, 0);
    load_chunk(1, 1);
    load_chunk(2, 2);
    load_chunk(3, 3);
#define K2_ITER(i, N)                                          \
  {                                                            \
    WAITV(N);                                                  \
    BARRIER();                                                 \
    if (i + 4 <= 15) load_chunk(i + 4, (i + 4) % K2_NSLOT);    \
  }
    K2_ITER(0, 21) K2_ITER(1, 21) K2_ITER(2, 21) K2_ITER(3, 21) K2_ITER(4, 21) K2_ITER(5, 21)
    K2_ITER(6, 21) K2_ITER(7, 21) K2_ITER(8, 21) K2_ITER(9, 21) K2_ITER(10, 21) K2_ITER(11, 21)
    K2_ITER(12, 21) K2_ITER(13, 14) K2_ITER(14, 7) K2_ITER(15, 0)
#undef K2_ITER
    return;
  }

  auto compute = [&](int slot) {
    const char* ol = lds + slot * K2_SLOT;
    const char* wl = ol + 12288;
#pragma unroll
    for (int s = 0; s < 2; ++s) {
      int r0 = 32 * s + 8 * g + tq;
      bf16x4 lo = TRREAD(wl + r0 * 224 + 32 * w + 8 * tp);
      bf16x4 hi = TRREAD(wl + (r0 + 4) * 224 + 32 * w + 8 * tp);
      bf16x8 af = __builtin_shufflevector(lo, hi, 0, 1, 2, 3, 4, 5, 6, 7);
#pragma unroll
      for (int rt = 0; rt < 6; ++rt) {
        int row = 16 * rt + l15;
        bf16x8 bfr = *reinterpret_cast<const bf16x8*>(ol + row * 128 + (((4 * s + g) ^ (row & 7)) << 4));
        acc[rt] = MFMA32(af, bfr, acc[rt]);
      }
    }
  };
#pragma unroll
  for (int i = 0; i < 16; ++i) {
    BARRIER();
    compute(i % K2_NSLOT);
  }

  float* pp = part + ((size_t)((blk * 4 + ks) * 768 + mt * 96 + l15)) * 448 + nt * 112 + 16 * w + 4 * g;
#pragma unroll
  for (int rt = 0; rt < 6; ++rt) *reinterpret_cast<f32x4*>(pp + (size_t)rt * 16 * 448) = acc[rt];
}

__global__ __launch_bounds__(256) void k3_reduce(const float* __restrict__ part, unsigned short* __restrict__ out) {
  int i = blockIdx.x * 256 + threadIdx.x;
  if (i >= 2 * 768 * 112) return;
  int q = i % 112;
  int row = (i / 112) % 768;
  int blk = i / (112 * 768);
  const float* p = part + ((size_t)(blk * 4) * 768 + row) * 448 + q * 4;
  f32x4 s = *reinterpret_cast<const f32x4*>(p);
#pragma unroll
  for (int k = 1; k < 4; ++k) s += *reinterpret_cast<const f32x4*>(p + (size_t)k * 768 * 448);
  u32x2 v;
  v[0] = pack2(s[0], s[1]);
  v[1] = pack2(s[2], s[3]);
  *reinterpret_cast<u32x2*>(out + (size_t)row * 896 + blk * 448 + q * 4) = v;
}

extern "C" void kernel_launch(void* const* d_in, const int* in_sizes, int n_in, void* d_out, int out_size,
                              void* d_ws, size_t ws_size, hipStream_t stream) {
  const unsigned short* xA = (const unsigned short*)d_in[0];
  const unsigned short* kA = (const unsigned short*)d_in[1];
  const unsigned short* vA = (const unsigned short*)d_in[2];
  const unsigned short* wqA = (const unsigned short*)d_in[3];
  const unsigned short* woA = (const unsigned short*)d_in[4];
  const unsigned short* xB = (const unsigned short*)d_in[5];
  const unsigned short* kB = (const unsigned short*)d_in[6];
  const unsigned short* vB = (const unsigned short*)d_in[7];
  const unsigned short* wqB = (const unsigned short*)d_in[8];
  const unsigned short* woB = (const unsigned short*)d_in[9];
  unsigned short* ows = (unsigned short*)d_ws;
  float* part = (float*)((char*)d_ws + (size_t)2 * 768 * 4096 * 2);
  k1_qproj_attn<<<256, 512, 0, stream>>>(xA, kA, vA, wqA, xB, kB, vB, wqB, ows, woA, woB);
  k2_oproj<<<256, 704, 0, stream>>>(ows, woA, woB, part);
  k3_reduce<<<(2 * 768 * 112 + 255) / 256, 256, 0, stream>>>(part, (unsigned short*)d_out);
}
```
